```python
import torch

def _softmax(x, axis=-1):
    return torch.softmax(x, dim=axis)
def _gelu(x):
    return torch.nn.functional.gelu(x, approximate="tanh")
def _silu(x):
    return torch.nn.functional.silu(x)
def _sigmoid(x):
    return torch.sigmoid(x)
def _layernorm(x, axis=-1, eps=1e-5):
    m = x.mean(dim=axis, keepdim=True)
    v = x.var(dim=axis, keepdim=True, unbiased=False)
    return (x - m) / torch.sqrt(v + eps)
def _scatter_add(tgt, idx, src, axis=0):
    return tgt.clone().index_add_(axis, idx, src)
def _segment_sum(src, seg, n):
    out = torch.zeros((n,) + src.shape[1:], dtype=src.dtype)
    return out.index_add_(0, seg, src)
def _pad(x, pw):
    flat = tuple(v for pair in reversed(pw) for v in pair)
    return torch.nn.functional.pad(x, flat)

kernel_name = 'concat_heads__gqa_block_cached__gqa_block_cached__decode'

def setup_inputs():

    _w = (lambda a: (lambda t: t.to(torch.bfloat16) if t.is_floating_point() else t)(torch.as_tensor(a)))
    def _A_setup():
        B, Sq, D = 4, 1, 448
        Hq, Hkv, Sk, Dh = 16, 8, 8192, 256
        assert Hq % Hkv == 0, (Hq, Hkv)
        return {
            "x": _w((lambda *s: torch.randn(*s, dtype=torch.bfloat16) * 0.8)(B, Sq, D)),
            "k_cache": _w((lambda *s: torch.randn(*s, dtype=torch.bfloat16) * 0.8)(B, Hkv, Sk, Dh)),
            "v_cache": _w((lambda *s: torch.randn(*s, dtype=torch.bfloat16) * 0.05)(B, Hkv, Sk, Dh)),
            "wq": _w((lambda *s: torch.randn(*s, dtype=torch.bfloat16) * 0.8)(D, Hq * Dh) * (D ** -0.5)),
            "wo": _w((lambda *s: torch.randn(*s, dtype=torch.bfloat16) * 0.05)(Hq * Dh, D) * ((Hq * Dh) ** -0.5)),
        }
    _A_in = _A_setup()

    def _B_setup():
        B, Sq, D = 4, 1, 448
        Hq, Hkv, Sk, Dh = 16, 8, 8192, 256
        assert Hq % Hkv == 0, (Hq, Hkv)
        return {
            "x": _w((lambda *s: torch.randn(*s, dtype=torch.bfloat16) * 0.8)(B, Sq, D)),
            "k_cache": _w((lambda *s: torch.randn(*s, dtype=torch.bfloat16) * 0.8)(B, Hkv, Sk, Dh)),
            "v_cache": _w((lambda *s: torch.randn(*s, dtype=torch.bfloat16) * 0.05)(B, Hkv, Sk, Dh)),
            "wq": _w((lambda *s: torch.randn(*s, dtype=torch.bfloat16) * 0.8)(D, Hq * Dh) * (D ** -0.5)),
            "wo": _w((lambda *s: torch.randn(*s, dtype=torch.bfloat16) * 0.05)(Hq * Dh, D) * ((Hq * Dh) ** -0.5)),
        }
    _B_in = _B_setup()

    return {
        "A_x": _A_in["x"],
        "A_k_cache": _A_in["k_cache"],
        "A_v_cache": _A_in["v_cache"],
        "A_wq": _A_in["wq"],
        "A_wo": _A_in["wo"],
        "B_x": _B_in["x"],
        "B_k_cache": _B_in["k_cache"],
        "B_v_cache": _B_in["v_cache"],
        "B_wq": _B_in["wq"],
        "B_wo": _B_in["wo"],
    }

def reference(A_x, A_k_cache, A_v_cache, A_wq, A_wo, B_x, B_k_cache, B_v_cache, B_wq, B_wo):
    def _A_ref(x, k_cache, v_cache, wq, wo):
        B, Sq, D = x.shape
        Hq, Dh = 16, 256
        Hkv = k_cache.shape[1]
        rep = Hq // Hkv
        q = (lambda x, *d: x.permute(*d))((x @ wq).reshape(B, Sq, Hq, Dh), 0, 2, 1, 3)
        k = (lambda x, n, axis: torch.repeat_interleave(x, n, dim=axis))(k_cache, rep, axis=1)
        v = (lambda x, n, axis: torch.repeat_interleave(x, n, dim=axis))(v_cache, rep, axis=1)
        scores = torch.einsum("bhqd,bhkd->bhqk", q, k) * (Dh ** -0.5)
        p = _softmax(scores, axis=-1)
        o = torch.einsum("bhqk,bhkd->bhqd", p, v)
        return (lambda x, *d: x.permute(*d))(o, 0, 2, 1, 3).reshape(B, Sq, Hq * Dh) @ wo

    def _B_ref(x, k_cache, v_cache, wq, wo):
        B, Sq, D = x.shape
        Hq, Dh = 16, 256
        Hkv = k_cache.shape[1]
        rep = Hq // Hkv
        q = (lambda x, *d: x.permute(*d))((x @ wq).reshape(B, Sq, Hq, Dh), 0, 2, 1, 3)
        k = (lambda x, n, axis: torch.repeat_interleave(x, n, dim=axis))(k_cache, rep, axis=1)
        v = (lambda x, n, axis: torch.repeat_interleave(x, n, dim=axis))(v_cache, rep, axis=1)
        scores = torch.einsum("bhqd,bhkd->bhqk", q, k) * (Dh ** -0.5)
        p = _softmax(scores, axis=-1)
        o = torch.einsum("bhqk,bhkd->bhqd", p, v)
        return (lambda x, *d: x.permute(*d))(o, 0, 2, 1, 3).reshape(B, Sq, Hq * Dh) @ wo

    a = _A_ref(A_x, A_k_cache, A_v_cache, A_wq, A_wo)
    b = _B_ref(B_x, B_k_cache, B_v_cache, B_wq, B_wo)
    return torch.concatenate([a, b], axis=-1)
```

```cpp
#include <hip/hip_runtime.h>
#include <hip/hip_bf16.h>
#include <stdint.h>

namespace {

typedef unsigned v4u __attribute__((ext_vector_type(4)));
typedef unsigned v2u __attribute__((ext_vector_type(2)));
typedef float f32x4 __attribute__((ext_vector_type(4)));
typedef float f32x2 __attribute__((ext_vector_type(2)));
typedef short v4s __attribute__((ext_vector_type(4)));
typedef __bf16 bf16x2 __attribute__((ext_vector_type(2)));
typedef __bf16 bf16x8 __attribute__((ext_vector_type(8)));
#define LAS __attribute__((address_space(3)))
#define SB() __builtin_amdgcn_sched_barrier(0)
#define MEMFENCE() asm volatile("" ::: "memory")

constexpr int DM = 448, NQ = 4096, SK = 8192, DH = 256;
constexpr unsigned REGION_BYTES = SK * DH * 2;
constexpr int NST = 64;
#define NSTAT 13
constexpr int AUX_NT = 2, AUX_SC1 = 16, RSRC_W3 = 0x00020000;
constexpr int OOB = 0x7ffffff0;
constexpr unsigned SPIN_CAP = 1u << 18;
constexpr float SCL = 0.0625f * 1.4426950408889634f;

constexpr unsigned WS_EPOCH = 0;
constexpr unsigned WS_CNT = 4096;
constexpr unsigned WS_ML = 16384;
constexpr unsigned WS_QG = 32768;
constexpr unsigned WS_OG = WS_QG + 2 * 4 * 4096 * 8;
constexpr unsigned WS_PG = WS_OG + 256 * 512 * 8;
constexpr unsigned WS_END = WS_PG + 2 * 4 * 28 * 32 * 16 * 8;

constexpr int L_WO = 0;
constexpr int L_MRG = 32768;
constexpr int L_QP = L_MRG + 8192;
constexpr int L_Q = L_QP + 2048;
constexpr int L_O = L_Q + 1024;
constexpr int L_ML = L_O + 1024;
constexpr int L_TK = L_ML + 64;
constexpr int L_BYTES = 84 * 1024;

struct Params {
  const void* x[2];
  const void* k[2];
  const void* v[2];
  const void* wq[2];
  const void* wo[2];
  void* out;
  unsigned char* ws;
};

__device__ __forceinline__ float dot2(unsigned a, unsigned b, float c) {
  return __builtin_amdgcn_fdot2_f32_bf16(__builtin_bit_cast(bf16x2, a), __builtin_bit_cast(bf16x2, b), c, false);
}
__device__ __forceinline__ unsigned pk_lo(unsigned a, unsigned b) { return __builtin_amdgcn_perm(b, a, 0x05040100u); }
__device__ __forceinline__ unsigned pk_hi(unsigned a, unsigned b) { return __builtin_amdgcn_perm(b, a, 0x07060302u); }
__device__ __forceinline__ unsigned cvtpk(float lo, float hi) {
  bf16x2 c = {(__bf16)lo, (__bf16)hi};
  return __builtin_bit_cast(unsigned, c);
}
template <int CTRL> __device__ __forceinline__ float dppf(float x) {
  return __builtin_bit_cast(float, __builtin_amdgcn_mov_dpp(__builtin_bit_cast(int, x), CTRL, 0xf, 0xf, true));
}
template <int CTRL> __device__ __forceinline__ unsigned dppu(unsigned x) {
  return (unsigned)__builtin_amdgcn_mov_dpp((int)x, CTRL, 0xf, 0xf, true);
}
constexpr int XOR1 = 0xB1, XOR2 = 0x4E, XOR7 = 0x141, ROR8 = 0x128, ROR4 = 0x124, ROR2 = 0x122, ROR1 = 0x121, BCAST = 0x150;
__device__ __forceinline__ float sum16(float a, float b) {
  auto r = __builtin_amdgcn_permlane16_swap(__float_as_uint(a), __float_as_uint(b), false, false);
  return __uint_as_float(r[0]) + __uint_as_float(r[1]);
}
__device__ __forceinline__ float sum32(float a, float b) {
  auto r = __builtin_amdgcn_permlane32_swap(__float_as_uint(a), __float_as_uint(b), false, false);
  return __uint_as_float(r[0]) + __uint_as_float(r[1]);
}
__device__ __forceinline__ float max32(float a) {
  auto r = __builtin_amdgcn_permlane32_swap(__float_as_uint(a), __float_as_uint(a), false, false);
  return fmaxf(__uint_as_float(r[0]), __uint_as_float(r[1]));
}
__device__ __forceinline__ unsigned enc_f(float f, unsigned taglo) { return __builtin_amdgcn_alignbit(taglo, __float_as_uint(f), 8); }
__device__ __forceinline__ float dec_f(unsigned lo) { return __uint_as_float(lo << 8); }
__device__ __forceinline__ unsigned bad2(v4u g, unsigned taglo, unsigned taghi) {
  return ((g.x ^ (taglo << 24)) | (g.z ^ (taglo << 24))) >> 24 | (g.y ^ taghi) | (g.w ^ taghi);
}

#define STAMP(i) do { } while (0)

__global__ void __launch_bounds__(256, 1) decode_fused(Params p) {
  __shared__ __attribute__((aligned(16))) unsigned char smem[L_BYTES];
  LAS unsigned char* L = (LAS unsigned char*)smem;
  STAMP(0);
  const int bid = blockIdx.x, grp = bid >> 4, j = bid & 15;
  const int br = grp >> 3, kvh = grp & 7, b = j >> 2, sp = j & 3;
  const int tid = threadIdx.x, lane = tid & 63, w = __builtin_amdgcn_readfirstlane(tid >> 6);
  const int qs = (j & 7) * 2 + (j >> 3);
  const int cb = (j & 1) * 2 + (j >> 3), rb = (j >> 1) & 3;

  const auto rws = __builtin_amdgcn_make_buffer_rsrc(p.ws, 0, WS_END, RSRC_W3);
  const auto rx = __builtin_amdgcn_make_buffer_rsrc((void*)p.x[br], 0, 4 * DM * 2, RSRC_W3);
  const auto rwq = __builtin_amdgcn_make_buffer_rsrc((void*)p.wq[br], 0, DM * NQ * 2, RSRC_W3);
  const auto rwo = __builtin_amdgcn_make_buffer_rsrc((void*)p.wo[br], 0, NQ * DM * 2, RSRC_W3);
  const auto rk = __builtin_amdgcn_make_buffer_rsrc((char*)p.k[br] + (size_t)(b * 8 + kvh) * REGION_BYTES, 0, REGION_BYTES, RSRC_W3);
  const auto rv = __builtin_amdgcn_make_buffer_rsrc((char*)p.v[br] + (size_t)(b * 8 + kvh) * REGION_BYTES, 0, REGION_BYTES, RSRC_W3);
  const int cnt_off = WS_CNT + (bid >> 2) * 128;

  __builtin_amdgcn_raw_buffer_store_b32(0, rws, (sp == 0 && tid == 0) ? 0 : OOB, cnt_off, AUX_SC1);
  const v2u ep = __builtin_amdgcn_raw_buffer_load_b64(rws, 0, WS_EPOCH + br * 256, AUX_SC1);
  v4u X[4], W[8], WO[8], KX[16], K[16], V[16];
  {
    const int row0 = 128 * w + 8 * (lane >> 2);
    const int xoff = row0 < DM ? row0 * 2 : OOB;
#pragma unroll
    for (int i = 0; i < 4; ++i) X[i] = __builtin_amdgcn_raw_buffer_load_b128(rx, xoff, i * DM * 2, 0);
    const int woff = row0 * (NQ * 2) + (lane & 3) * 16;
#pragma unroll
    for (int n = 0; n < 8; ++n) W[n] = __builtin_amdgcn_raw_buffer_load_b128(rwq, woff, (kvh * 512 + qs * 32) * 2 + n * (NQ * 2), AUX_NT);
  }
  SB();
  {
    const int ooff = (lane & 15) < 14 ? (lane >> 4) * (DM * 2) + (lane & 15) * 16 : OOB;
#pragma unroll
    for (int n = 0; n < 8; ++n)
      WO[n] = __builtin_amdgcn_raw_buffer_load_b128(rwo, ooff, (kvh * 512 + rb * 128 + 32 * w + 4 * n) * (DM * 2) + cb * 224, AUX_NT);
  }
  SB();
  const int voff = lane * 16;
#define LDK(i, st) __builtin_amdgcn_raw_buffer_load_b128(rk, voff, (st) * 65536 + w * 1024 + (i) * 4096, AUX_NT)
#define LDV(i, st) __builtin_amdgcn_raw_buffer_load_b128(rv, voff, (st) * 65536 + w * 1024 + (i) * 4096, AUX_NT)
#pragma unroll
  for (int i = 0; i < 16; ++i) KX[i] = LDK(i, sp);
  SB();
  if (tid < 64) *(LAS unsigned*)(L + L_TK + tid * 4) = 0u;

  {
    float a[4][8];
#pragma unroll
    for (int bb = 0; bb < 4; ++bb)
#pragma unroll
      for (int c = 0; c < 8; ++c) a[bb][c] = 0.f;
#pragma unroll
    for (int t = 0; t < 4; ++t)
#pragma unroll
      for (int k = 0; k < 4; ++k) {
        const unsigned lo = pk_lo(W[2 * t][k], W[2 * t + 1][k]), hi = pk_hi(W[2 * t][k], W[2 * t + 1][k]);
#pragma unroll
        for (int bb = 0; bb < 4; ++bb) {
          a[bb][2 * k] = dot2(lo, X[bb][t], a[bb][2 * k]);
          a[bb][2 * k + 1] = dot2(hi, X[bb][t], a[bb][2 * k + 1]);
        }
      }
    float y[8];
#pragma unroll
    for (int c = 0; c < 8; ++c) {
      y[c] = sum16(sum32(a[0][c], a[2][c]), sum32(a[1][c], a[3][c]));
      y[c] += dppf<ROR8>(y[c]);
      y[c] += dppf<ROR4>(y[c]);
    }
    LAS f32x4* qp = (LAS f32x4*)(L + L_QP + w * 512 + (lane >> 4) * 128 + (lane & 3) * 32);
    qp[0] = f32x4{y[0], y[1], y[2], y[3]};
    qp[1] = f32x4{y[4], y[5], y[6], y[7]};
  }
  STAMP(1);
  __syncthreads();
  const unsigned long long tag = (((unsigned long long)ep.y << 32) | ep.x) + 1ull;
  const unsigned taglo = __builtin_amdgcn_readfirstlane((unsigned)tag & 0xffu), taghi = __builtin_amdgcn_readfirstlane((unsigned)(tag >> 8));
  {
    const LAS float* qp = (const LAS float*)(L + L_QP + w * 128 + (lane & 31) * 4);
    const float q = (qp[0] + qp[128]) + (qp[256] + qp[384]);
    const v2u g = {(cvtpk(q, q) & 0xffffu) | (taglo << 24), taghi};
    __builtin_amdgcn_raw_buffer_store_b64(g, rws, lane < 32 ? lane * 8 : OOB, WS_QG + ((br * 4 + w) * NQ + kvh * 512 + qs * 32) * 8, AUX_SC1);
  }
  SB();
#pragma unroll
  for (int i = 0; i < 8; ++i) V[i] = LDV(i, sp);
  SB();
#pragma unroll
  for (int n = 0; n < 8; ++n) *(LAS v4u*)(L + L_WO + (32 * w + 4 * n) * 256 + lane * 16) = WO[n];
  SB();
  STAMP(2);
#define POLL_AT 17
#define STR2(x) #x
#define STR(x) STR2(x)
  asm volatile("s_waitcnt vmcnt(" STR(POLL_AT) ")" ::: "memory");
  const int qg_off = WS_QG + ((br * 4 + b) * NQ + kvh * 512) * 8;
  v4u qg = __builtin_amdgcn_raw_buffer_load_b128(rws, tid * 16, qg_off, AUX_SC1);
  STAMP(3);
  SB();
#pragma unroll
  for (int i = 8; i < 16; ++i) V[i] = LDV(i, sp);
  SB();
#pragma unroll
  for (int i = 0; i < 16; ++i) K[i] = LDK(i, 4 + sp);
  SB();
  if (__any(bad2(qg, taglo, taghi))) {
#pragma unroll 1
    for (unsigned spins = 0; spins < SPIN_CAP; ++spins) {
      MEMFENCE();
      qg = __builtin_amdgcn_raw_buffer_load_b128(rws, tid * 16, qg_off, AUX_SC1);
      if (!__any(bad2(qg, taglo, taghi))) break;
      __builtin_amdgcn_s_sleep(2);
    }
  }
  *(LAS unsigned*)(L + L_Q + tid * 4) = pk_lo(qg.x, qg.z);
  STAMP(4);
  __syncthreads();
  const v4u q0 = *(const LAS v4u*)(L + L_Q + (lane & 31) * 16), q1 = *(const LAS v4u*)(L + L_Q + 512 + (lane & 31) * 16);

  float acc[2][8], m = -INFINITY, l = 0.f;
#pragma unroll
  for (int k = 0; k < 8; ++k) acc[0][k] = acc[1][k] = 0.f;
  const bool b3 = lane & 8, b2 = lane & 4, b1 = lane & 2, b0 = lane & 1;

#define QK_STEP(KR, i)                                                 \
  {                                                                    \
    float s0 = dot2(KR[i][0], q0[0], 0.f), s1 = dot2(KR[i][0], q1[0], 0.f); \
    s0 = dot2(KR[i][1], q0[1], s0); s1 = dot2(KR[i][1], q1[1], s1);    \
    s0 = dot2(KR[i][2], q0[2], s0); s1 = dot2(KR[i][2], q1[2], s1);    \
    s0 = dot2(KR[i][3], q0[3], s0); s1 = dot2(KR[i][3], q1[3], s1);    \
    y[i] = sum16(s0, s1);                                              \
  }
#define TR_STEP(H, CTRL, BIT)                                          \
  _Pragma("unroll") for (int i = 0; i < (H); ++i) {                    \
    const float keep = (BIT) ? y[i + (H)] : y[i], send = (BIT) ? y[i] : y[i + (H)]; \
    y[i] = keep + dppf<CTRL>(send);                                    \
  }
#define SOFTMAX()                                                      \
  TR_STEP(8, ROR8, b3) TR_STEP(4, XOR7, b2) TR_STEP(2, XOR2, b1) TR_STEP(1, XOR1, b0) \
  float tm = y[0];                                                     \
  tm = fmaxf(tm, dppf<ROR8>(tm)); tm = fmaxf(tm, dppf<ROR4>(tm)); tm = fmaxf(tm, dppf<ROR2>(tm)); tm = fmaxf(tm, dppf<ROR1>(tm)); \
  const float mn = fmaxf(m, max32(tm));                                \
  const float al = __builtin_amdgcn_exp2f((m - mn) * SCL), pp = __builtin_amdgcn_exp2f((y[0] - mn) * SCL); \
  m = mn;                                                              \
  l = fmaf(l, al, pp);                                                 \
  const float al0 = __builtin_bit_cast(float, __builtin_amdgcn_readlane(__builtin_bit_cast(int, al), 0));  \
  const float al1 = __builtin_bit_cast(float, __builtin_amdgcn_readlane(__builtin_bit_cast(int, al), 16)); \
  _Pragma("unroll") for (int k = 0; k < 8; ++k) { acc[0][k] *= al0; acc[1][k] *= al1; } \
  const unsigned ppk = cvtpk(pp, dppf<XOR1>(pp));     \
  const auto ph = __builtin_amdgcn_permlane16_swap(ppk, ppk, false, false);
#define PV_STEP(t)                                                     \
  {                                                                    \
    const unsigned p0 = dppu<BCAST + 2 * (t)>(ph[0]), p1 = dppu<BCAST + 2 * (t)>(ph[1]); \
    _Pragma("unroll") for (int k = 0; k < 4; ++k) {                    \
      const unsigned lo = pk_lo(V[2 * (t)][k], V[2 * (t) + 1][k]), hi = pk_hi(V[2 * (t)][k], V[2 * (t) + 1][k]); \
      acc[0][2 * k] = dot2(lo, p0, acc[0][2 * k]); acc[0][2 * k + 1] = dot2(hi, p0, acc[0][2 * k + 1]); \
      acc[1][2 * k] = dot2(lo, p1, acc[1][2 * k]); acc[1][2 * k + 1] = dot2(hi, p1, acc[1][2 * k + 1]); \
    }                                                                  \
    V[2 * (t)] = LDV(2 * (t), nxt); V[2 * (t) + 1] = LDV(2 * (t) + 1, nxt); \
    SB();                                                              \
  }
#define PV_ALL() PV_STEP(0) PV_STEP(1) PV_STEP(2) PV_STEP(3) PV_STEP(4) PV_STEP(5) PV_STEP(6) PV_STEP(7)

  {
    float y[16];
    const int nxt = 4 + sp;
#pragma unroll
    for (int i = 0; i < 16; ++i) QK_STEP(KX, i)
    SOFTMAX()
    SB();
    PV_ALL()
  }
  v4u BW[2][4];
#pragma unroll
  for (int c = 0; c < 2; ++c)
#pragma unroll
    for (int ks = 0; ks < 4; ++ks) {
      LAS unsigned char* a = L + L_WO + (32 * ks + 8 * (lane >> 4) + ((lane >> 2) & 3)) * 256 + (2 * w + c) * 32 + (lane & 3) * 8;
      const v2u r0 = __builtin_bit_cast(v2u, __builtin_amdgcn_ds_read_tr16_b64_v4i16((LAS v4s*)a));
      const v2u r1 = __builtin_bit_cast(v2u, __builtin_amdgcn_ds_read_tr16_b64_v4i16((LAS v4s*)(a + 1024)));
      BW[c][ks] = v4u{r0.x, r0.y, r1.x, r1.y};
    }
  STAMP(5);
  SB();
  {
    volatile LAS unsigned* tks = (volatile LAS unsigned*)(L + L_TK);
    const int aoff = tid == 0 ? 0 : OOB;
    unsigned n = 1, nxt;
#pragma unroll 1
    do {
      nxt = (n + 1) * 4 + sp;
      if (n + 1 >= NSTAT) {
        unsigned t = 0;
#pragma unroll 1
        for (unsigned spins = 0; spins < SPIN_CAP; ++spins) {
          t = __builtin_amdgcn_readfirstlane(tks[n + 1]);
          if (t) break;
        }
        nxt = min(t - 1u, (unsigned)NST) + 4 * NSTAT;
        if (n >= 40) nxt = NST;
      }
      const bool req = n + 2 >= NSTAT && nxt < NST;
      const unsigned tk = __builtin_amdgcn_raw_ptr_buffer_atomic_add_i32(1, rws, req ? aoff : OOB, cnt_off, AUX_SC1);
      SB();
      float y[16];
#pragma unroll
      for (int i = 0; i < 16; ++i) {
        QK_STEP(K, i)
        K[i] = LDK(i, nxt);
        SB();
      }
      SOFTMAX()
      SB();
      PV_ALL()
      tks[(req && w == 0) ? n + 2 : 63] = __builtin_amdgcn_readfirstlane(tk) + 1u;
      SB();
      ++n;
    } while (nxt < NST);
  }
  STAMP(6);

  {
    float o[8];
#pragma unroll
    for (int k = 0; k < 8; ++k) o[k] = sum32(acc[0][k], acc[1][k]);
    LAS f32x4* mp = (LAS f32x4*)(L + L_MRG + w * 2048 + lane * 32);
    mp[0] = f32x4{o[0], o[1], o[2], o[3]};
    mp[1] = f32x4{o[4], o[5], o[6], o[7]};
    l += dppf<ROR8>(l); l += dppf<ROR4>(l); l += dppf<ROR2>(l); l += dppf<ROR1>(l);
    l = sum32(l, l);
    *(LAS f32x2*)(L + L_ML + w * 16 + ((lane >> 4) & 1) * 8) = f32x2{m * SCL, l};
  }
  __syncthreads();
  {
    f32x2 ml[4], ov[4];
#pragma unroll
    for (int i = 0; i < 4; ++i) {
      ml[i] = *(const LAS f32x2*)(L + L_ML + i * 16 + (w >> 1) * 8);
      ov[i] = *(const LAS f32x2*)(L + L_MRG + i * 2048 + tid * 8);
    }
    const float M = __uint_as_float(__float_as_uint(fmaxf(fmaxf(ml[0].x, ml[1].x), fmaxf(ml[2].x, ml[3].x))) & 0xffffff00u);
    float o0 = 0.f, o1 = 0.f, ls = 0.f;
#pragma unroll
    for (int i = 0; i < 4; ++i) {
      const float e = __builtin_amdgcn_exp2f(ml[i].x - M);
      o0 = fmaf(e, ov[i].x, o0); o1 = fmaf(e, ov[i].y, o1); ls = fmaf(e, ml[i].y, ls);
    }
    __builtin_amdgcn_raw_buffer_store_b128(v4u{enc_f(o0, taglo), taghi, enc_f(o1, taglo), taghi}, rws, tid * 16, WS_OG + bid * 4096, AUX_SC1);
    __builtin_amdgcn_raw_buffer_store_b128(v4u{enc_f(M, taglo), taghi, enc_f(ls, taglo), taghi}, rws, (tid & 127) == 0 ? 0 : OOB,
                                           WS_ML + (bid >> 2) * 128 + (w >> 1) * 64 + sp * 16, AUX_SC1);
  }
  STAMP(7);

  {
    v4u g[4], gm;
    const int og_off = WS_OG + (grp * 16 + w * 4) * 4096 + rb * 1024, ml_off = WS_ML + (grp * 4 + w) * 128 + (rb >> 1) * 64;
#pragma unroll 1
    for (unsigned spins = 0; spins < SPIN_CAP; ++spins) {
      MEMFENCE();
      gm = __builtin_amdgcn_raw_buffer_load_b128(rws, (lane & 3) * 16, ml_off, AUX_SC1);
#pragma unroll
      for (int s = 0; s < 4; ++s) g[s] = __builtin_amdgcn_raw_buffer_load_b128(rws, voff, og_off + s * 4096, AUX_SC1);
      if (!__any(bad2(gm, taglo, taghi) | bad2(g[0], taglo, taghi) | bad2(g[1], taglo, taghi) | bad2(g[2], taglo, taghi) | bad2(g[3], taglo, taghi))) break;
      __builtin_amdgcn_s_sleep(4);
    }
    const float Ms = dec_f(gm.x), Ls = dec_f(gm.z);
    float Mx = fmaxf(Ms, dppf<XOR1>(Ms));
    Mx = fmaxf(Mx, dppf<XOR2>(Mx));
    const float e = __builtin_amdgcn_exp2f(Ms - Mx);
    float Lt = e * Ls;
    Lt += dppf<XOR1>(Lt);
    Lt += dppf<XOR2>(Lt);
    const float c = e * __builtin_amdgcn_rcpf(Lt);
    const float c0 = dppf<0x00>(c), c1 = dppf<0x55>(c), c2 = dppf<0xAA>(c), c3 = dppf<0xFF>(c);
    const float o0 = fmaf(c0, dec_f(g[0].x), c1 * dec_f(g[1].x)) + fmaf(c2, dec_f(g[2].x), c3 * dec_f(g[3].x));
    const float o1 = fmaf(c0, dec_f(g[0].z), c1 * dec_f(g[1].z)) + fmaf(c2, dec_f(g[2].z), c3 * dec_f(g[3].z));
    *(LAS unsigned*)(L + L_O + w * 256 + lane * 4) = cvtpk(o0, o1);
  }
  STAMP(8);
  __syncthreads();
  {
    f32x4 d0 = {0.f, 0.f, 0.f, 0.f}, d1 = {0.f, 0.f, 0.f, 0.f};
#pragma unroll
    for (int ks = 0; ks < 4; ++ks) {
      const v4u a = *(const LAS v4u*)(L + L_O + ((lane & 15) >> 2) * 256 + ks * 64 + (lane >> 4) * 16);
      d0 = __builtin_amdgcn_mfma_f32_16x16x32_bf16(__builtin_bit_cast(bf16x8, a), __builtin_bit_cast(bf16x8, BW[0][ks]), d0, 0, 0, 0);
      d1 = __builtin_amdgcn_mfma_f32_16x16x32_bf16(__builtin_bit_cast(bf16x8, a), __builtin_bit_cast(bf16x8, BW[1][ks]), d1, 0, 0, 0);
    }
    const int po = (lane >> 4) * (28 * 4096) + (lane & 15) * 8;
    const int pg_off = WS_PG + (br * 4 * 28 + cb * 7 + 2 * w) * 4096 + (kvh * 4 + rb) * 128;
    __builtin_amdgcn_raw_buffer_store_b64(v2u{enc_f(d0[0], taglo), taghi}, rws, po, pg_off, AUX_SC1);
    __builtin_amdgcn_raw_buffer_store_b64(v2u{enc_f(d1[0], taglo), taghi}, rws, w < 3 ? po : OOB, pg_off + 4096, AUX_SC1);
  }
  STAMP(9);

  const int ri = bid & 127;
  if (w == 3 && ri < 112) {
    v4u g[4];
    const int pg_off = WS_PG + (br * 112 + ri) * 4096;
#pragma unroll 1
    for (unsigned spins = 0; spins < SPIN_CAP; ++spins) {
      MEMFENCE();
#pragma unroll
      for (int s = 0; s < 4; ++s) g[s] = __builtin_amdgcn_raw_buffer_load_b128(rws, voff + s * 1024, pg_off, AUX_SC1);
      if (!__any(bad2(g[0], taglo, taghi) | bad2(g[1], taglo, taghi) | bad2(g[2], taglo, taghi) | bad2(g[3], taglo, taghi))) break;
      __builtin_amdgcn_s_sleep(2);
    }
    float s0 = (dec_f(g[0].x) + dec_f(g[1].x)) + (dec_f(g[2].x) + dec_f(g[3].x));
    float s1 = (dec_f(g[0].z) + dec_f(g[1].z)) + (dec_f(g[2].z) + dec_f(g[3].z));
    s0 += dppf<ROR8>(s0); s1 += dppf<ROR8>(s1);
    s0 = sum16(s0, s0); s1 = sum16(s1, s1);
    s0 = sum32(s0, s0); s1 = sum32(s1, s1);
    const auto ro = __builtin_amdgcn_make_buffer_rsrc(p.out, 0, 4 * 2 * DM * 2, RSRC_W3);
    const int ob = ri / 28, ct = ri - ob * 28;
    __builtin_amdgcn_raw_buffer_store_b32(cvtpk(s0, s1), ro, lane < 8 ? lane * 4 : OOB, (ob * 2 * DM + br * DM + ct * 16) * 2, 0);
    __builtin_amdgcn_raw_buffer_store_b64(v2u{(unsigned)tag, (unsigned)(tag >> 32)}, rws, (ri == 0 && lane == 0) ? 0 : OOB, WS_EPOCH + br * 256, AUX_SC1);
  }
}

}

extern "C" void kernel_launch(void* const* d_in, const int* in_sizes, int n_in, void* d_out, int out_size, void* d_ws, size_t ws_size,
                              hipStream_t stream) {
  Params p;
  for (int i = 0; i < 2; ++i) {
    p.x[i] = d_in[5 * i + 0];
    p.k[i] = d_in[5 * i + 1];
    p.v[i] = d_in[5 * i + 2];
    p.wq[i] = d_in[5 * i + 3];
    p.wo[i] = d_in[5 * i + 4];
  }
  p.out = d_out;
  p.ws = (unsigned char*)d_ws;
  decode_fused<<<256, 256, 0, stream>>>(p);
}
```

```cpp
#include <hip/hip_runtime.h>
#include <hip/hip_bf16.h>
#include <stdint.h>
#include <stdio.h>
#define H_A 0
#define NSTAT 10

namespace {

typedef unsigned v4u __attribute__((ext_vector_type(4)));
typedef unsigned v2u __attribute__((ext_vector_type(2)));
typedef float f32x4 __attribute__((ext_vector_type(4)));
typedef float f32x2 __attribute__((ext_vector_type(2)));
typedef short v4s __attribute__((ext_vector_type(4)));
typedef __bf16 bf16x2 __attribute__((ext_vector_type(2)));
typedef __bf16 bf16x8 __attribute__((ext_vector_type(8)));
#define LAS __attribute__((address_space(3)))
#define SB() __builtin_amdgcn_sched_barrier(0)
#define MEMFENCE() asm volatile("" ::: "memory")

constexpr int DM = 448, NQ = 4096, SK = 8192, DH = 256;
constexpr unsigned REGION_BYTES = SK * DH * 2;
constexpr int NST = 64;
constexpr int AUX_NT = 2, AUX_SC1 = 16, RSRC_W3 = 0x00020000;
constexpr int OOB = 0x7ffffff0;
constexpr unsigned SPIN_CAP = 1u << 18;
constexpr float SCL = 0.0625f * 1.4426950408889634f;

constexpr unsigned WS_EPOCH = 0;
constexpr unsigned WS_CNT = 4096;
constexpr unsigned WS_ML = 16384;
constexpr unsigned WS_QG = 32768;
constexpr unsigned WS_OG = WS_QG + 2 * 4 * 4096 * 8;
constexpr unsigned WS_PG = WS_OG + 256 * 512 * 8;
constexpr unsigned WS_END = WS_PG + 2 * 4 * 28 * 32 * 16 * 8;

constexpr int L_WO = 0;
constexpr int L_MRG = 32768;
constexpr int L_QP = L_MRG + 8192;
constexpr int L_Q = L_QP + 2048;
constexpr int L_O = L_Q + 1024;
constexpr int L_ML = L_O + 1024;
constexpr int L_TK = L_ML + 64;
constexpr int L_BYTES = 84 * 1024;

struct Params {
  const void* x[2];
  const void* k[2];
  const void* v[2];
  const void* wq[2];
  const void* wo[2];
  void* out;
  unsigned char* ws;
};

__device__ __forceinline__ float dot2(unsigned a, unsigned b, float c) {
  return __builtin_amdgcn_fdot2_f32_bf16(__builtin_bit_cast(bf16x2, a), __builtin_bit_cast(bf16x2, b), c, false);
}
__device__ __forceinline__ unsigned pk_lo(unsigned a, unsigned b) { return __builtin_amdgcn_perm(b, a, 0x05040100u); }
__device__ __forceinline__ unsigned pk_hi(unsigned a, unsigned b) { return __builtin_amdgcn_perm(b, a, 0x07060302u); }
__device__ __forceinline__ unsigned cvtpk(float lo, float hi) {
  bf16x2 c = {(__bf16)lo, (__bf16)hi};
  return __builtin_bit_cast(unsigned, c);
}
template <int CTRL> __device__ __forceinline__ float dppf(float x) {
  return __builtin_bit_cast(float, __builtin_amdgcn_mov_dpp(__builtin_bit_cast(int, x), CTRL, 0xf, 0xf, true));
}
template <int CTRL> __device__ __forceinline__ unsigned dppu(unsigned x) {
  return (unsigned)__builtin_amdgcn_mov_dpp((int)x, CTRL, 0xf, 0xf, true);
}
constexpr int XOR1 = 0xB1, XOR2 = 0x4E, XOR7 = 0x141, ROR8 = 0x128, ROR4 = 0x124, ROR2 = 0x122, ROR1 = 0x121, BCAST = 0x150;
__device__ __forceinline__ float sum16(float a, float b) {
  auto r = __builtin_amdgcn_permlane16_swap(__float_as_uint(a), __float_as_uint(b), false, false);
  return __uint_as_float(r[0]) + __uint_as_float(r[1]);
}
__device__ __forceinline__ float sum32(float a, float b) {
  auto r = __builtin_amdgcn_permlane32_swap(__float_as_uint(a), __float_as_uint(b), false, false);
  return __uint_as_float(r[0]) + __uint_as_float(r[1]);
}
__device__ __forceinline__ float max32(float a) {
  auto r = __builtin_amdgcn_permlane32_swap(__float_as_uint(a), __float_as_uint(a), false, false);
  return fmaxf(__uint_as_float(r[0]), __uint_as_float(r[1]));
}
__device__ __forceinline__ unsigned enc_f(float f, unsigned taglo) { return __builtin_amdgcn_alignbit(taglo, __float_as_uint(f), 8); }
__device__ __forceinline__ float dec_f(unsigned lo) { return __uint_as_float(lo << 8); }
__device__ __forceinline__ unsigned bad2(v4u g, unsigned taglo, unsigned taghi) {
  return ((g.x ^ (taglo << 24)) | (g.z ^ (taglo << 24))) >> 24 | (g.y ^ taghi) | (g.w ^ taghi);
}

#define STAMP(i) do { } while (0)

__global__ void __launch_bounds__(256, 1) decode_fused(Params p) {
  __shared__ __attribute__((aligned(16))) unsigned char smem[L_BYTES];
  LAS unsigned char* L = (LAS unsigned char*)smem;
  STAMP(0);
  const int bid = blockIdx.x, grp = bid >> 4, j = bid & 15;
  const int br = grp >> 3, kvh = grp & 7, b = j >> 2, sp = j & 3;
  const int tid = threadIdx.x, lane = tid & 63, w = __builtin_amdgcn_readfirstlane(tid >> 6);
  const int qs = (j & 7) * 2 + (j >> 3);
  const int cb = (j & 1) * 2 + (j >> 3), rb = (j >> 1) & 3;

  const auto rws = __builtin_amdgcn_make_buffer_rsrc(p.ws, 0, WS_END, RSRC_W3);
  const auto rx = __builtin_amdgcn_make_buffer_rsrc((void*)p.x[br], 0, 4 * DM * 2, RSRC_W3);
  const auto rwq = __builtin_amdgcn_make_buffer_rsrc((void*)p.wq[br], 0, DM * NQ * 2, RSRC_W3);
  const auto rwo = __builtin_amdgcn_make_buffer_rsrc((void*)p.wo[br], 0, NQ * DM * 2, RSRC_W3);
  const auto rk = __builtin_amdgcn_make_buffer_rsrc((char*)p.k[br] + (size_t)(b * 8 + kvh) * REGION_BYTES, 0, REGION_BYTES, RSRC_W3);
  const auto rv = __builtin_amdgcn_make_buffer_rsrc((char*)p.v[br] + (size_t)(b * 8 + kvh) * REGION_BYTES, 0, REGION_BYTES, RSRC_W3);
  const int cnt_off = WS_CNT + (bid >> 2) * 128;

  __builtin_amdgcn_raw_buffer_store_b32(0, rws, (sp == 0 && tid == 0) ? 0 : OOB, cnt_off, AUX_SC1);
  const v2u ep = __builtin_amdgcn_raw_buffer_load_b64(rws, 0, WS_EPOCH + br * 256, AUX_SC1);
  v4u X[4], W[8], WO[8], KX[16], K[16], V[16];
  {
    const int row0 = 128 * w + 8 * (lane >> 2);
    const int xoff = row0 < DM ? row0 * 2 : OOB;
#pragma unroll
    for (int i = 0; i < 4; ++i) X[i] = __builtin_amdgcn_raw_buffer_load_b128(rx, xoff, i * DM * 2, 0);
    const int woff = row0 * (NQ * 2) + (lane & 3) * 16;
#pragma unroll
    for (int n = 0; n < 8; ++n) W[n] = __builtin_amdgcn_raw_buffer_load_b128(rwq, woff, (kvh * 512 + qs * 32) * 2 + n * (NQ * 2), AUX_NT);
  }
  SB();
  {
    const int ooff = (lane & 15) < 14 ? (lane >> 4) * (DM * 2) + (lane & 15) * 16 : OOB;
#pragma unroll
    for (int n = 0; n < 8; ++n)
      WO[n] = __builtin_amdgcn_raw_buffer_load_b128(rwo, ooff, (kvh * 512 + rb * 128 + 32 * w + 4 * n) * (DM * 2) + cb * 224, AUX_NT);
  }
  SB();
  const int voff = lane * 16;
#define LDK(i, st) __builtin_amdgcn_raw_buffer_load_b128(rk, voff, (st) * 65536 + w * 1024 + (i) * 4096, AUX_NT)
#define LDV(i, st) __builtin_amdgcn_raw_buffer_load_b128(rv, voff, (st) * 65536 + w * 1024 + (i) * 4096, AUX_NT)
#define ISSUE(lo, hi)                                                                     \
  _Pragma("unroll") for (int i_ = (lo); i_ < (hi) && i_ < 48; ++i_) {                    \
    if (i_ < 16) KX[i_ & 15] = LDK(i_ & 15, sp);                                          \
    else if (i_ < 32) V[i_ & 15] = LDV(i_ & 15, sp);                                      \
    else K[i_ & 15] = LDK(i_ & 15, 4 + sp);                                               \
  }                                                                                       \
  SB();
#define H_D 4
#define H_P 16
#define H_C 26
  STAMP(12);
  ISSUE(0, H_A)
  if (tid < 64) *(LAS unsigned*)(L + L_TK + tid * 4) = 0u;
  STAMP(13);

  {
    float a[4][8];
#pragma unroll
    for (int bb = 0; bb < 4; ++bb)
#pragma unroll
      for (int c = 0; c < 8; ++c) a[bb][c] = 0.f;
#pragma unroll
    for (int t = 0; t < 4; ++t) {
#pragma unroll
      for (int k = 0; k < 4; ++k) {
        const unsigned lo = pk_lo(W[2 * t][k], W[2 * t + 1][k]), hi = pk_hi(W[2 * t][k], W[2 * t + 1][k]);
#pragma unroll
        for (int bb = 0; bb < 4; ++bb) {
          a[bb][2 * k] = dot2(lo, X[bb][t], a[bb][2 * k]);
          a[bb][2 * k + 1] = dot2(hi, X[bb][t], a[bb][2 * k + 1]);
        }
      }
      SB();
      ISSUE(H_A + t, H_A + t + 1)
    }
    STAMP(14);
    float y[8];
#pragma unroll
    for (int c = 0; c < 8; ++c) {
      y[c] = sum16(sum32(a[0][c], a[2][c]), sum32(a[1][c], a[3][c]));
      y[c] += dppf<ROR8>(y[c]);
      y[c] += dppf<ROR4>(y[c]);
    }
    LAS f32x4* qp = (LAS f32x4*)(L + L_QP + w * 512 + (lane >> 4) * 128 + (lane & 3) * 32);
    qp[0] = f32x4{y[0], y[1], y[2], y[3]};
    qp[1] = f32x4{y[4], y[5], y[6], y[7]};
  }
  STAMP(1);
  __syncthreads();
  const unsigned long long tag = (((unsigned long long)ep.y << 32) | ep.x) + 1ull;
  const unsigned taglo = __builtin_amdgcn_readfirstlane((unsigned)tag & 0xffu), taghi = __builtin_amdgcn_readfirstlane((unsigned)(tag >> 8));
  {
    const LAS float* qp = (const LAS float*)(L + L_QP + w * 128 + (lane & 31) * 4);
    const float q = (qp[0] + qp[128]) + (qp[256] + qp[384]);
    const v2u g = {(cvtpk(q, q) & 0xffffu) | (taglo << 24), taghi};
    __builtin_amdgcn_raw_buffer_store_b64(g, rws, lane < 32 ? lane * 8 : OOB, WS_QG + ((br * 4 + w) * NQ + kvh * 512 + qs * 32) * 8, AUX_SC1);
  }
  SB();
  STAMP(2);
  ISSUE(H_A + 4, H_P)
  const int qg_off = WS_QG + ((br * 4 + b) * NQ + kvh * 512) * 8;
  v4u qg = __builtin_amdgcn_raw_buffer_load_b128(rws, tid * 16, qg_off, AUX_SC1);
  SB();
  STAMP(3);
#pragma unroll
  for (int n = 0; n < 8; ++n) *(LAS v4u*)(L + L_WO + (32 * w + 4 * n) * 256 + lane * 16) = WO[n];
  SB();
  ISSUE(H_P, H_C)
  STAMP(15);
  if (__any(bad2(qg, taglo, taghi))) {
#pragma unroll 1
    for (unsigned spins = 0; spins < SPIN_CAP; ++spins) {
      MEMFENCE();
      qg = __builtin_amdgcn_raw_buffer_load_b128(rws, tid * 16, qg_off, AUX_SC1);
      if (!__any(bad2(qg, taglo, taghi))) break;
      __builtin_amdgcn_s_sleep(2);
    }
  }
  *(LAS unsigned*)(L + L_Q + tid * 4) = pk_lo(qg.x, qg.z);
  STAMP(4);
  SB();
  ISSUE(H_C, H_C + 2)
  __syncthreads();
  const v4u q0 = *(const LAS v4u*)(L + L_Q + (lane & 31) * 16), q1 = *(const LAS v4u*)(L + L_Q + 512 + (lane & 31) * 16);

  float acc[2][8], m = -INFINITY, l = 0.f;
#pragma unroll
  for (int k = 0; k < 8; ++k) acc[0][k] = acc[1][k] = 0.f;
  const bool b3 = lane & 8, b2 = lane & 4, b1 = lane & 2, b0 = lane & 1;

#define QK_STEP(KR, i)                                                 \
  {                                                                    \
    float s0 = dot2(KR[i][0], q0[0], 0.f), s1 = dot2(KR[i][0], q1[0], 0.f); \
    s0 = dot2(KR[i][1], q0[1], s0); s1 = dot2(KR[i][1], q1[1], s1);    \
    s0 = dot2(KR[i][2], q0[2], s0); s1 = dot2(KR[i][2], q1[2], s1);    \
    s0 = dot2(KR[i][3], q0[3], s0); s1 = dot2(KR[i][3], q1[3], s1);    \
    y[i] = sum16(s0, s1);                                              \
  }
#define TR_STEP(H, CTRL, BIT)                                          \
  _Pragma("unroll") for (int i = 0; i < (H); ++i) {                    \
    const float keep = (BIT) ? y[i + (H)] : y[i], send = (BIT) ? y[i] : y[i + (H)]; \
    y[i] = keep + dppf<CTRL>(send);                                    \
  }
#define SOFTMAX()                                                      \
  TR_STEP(8, ROR8, b3) TR_STEP(4, XOR7, b2) TR_STEP(2, XOR2, b1) TR_STEP(1, XOR1, b0) \
  float tm = y[0];                                                     \
  tm = fmaxf(tm, dppf<ROR8>(tm)); tm = fmaxf(tm, dppf<ROR4>(tm)); tm = fmaxf(tm, dppf<ROR2>(tm)); tm = fmaxf(tm, dppf<ROR1>(tm)); \
  const float mn = fmaxf(m, max32(tm));                                \
  const float al = __builtin_amdgcn_exp2f((m - mn) * SCL), pp = __builtin_amdgcn_exp2f((y[0] - mn) * SCL); \
  m = mn;                                                              \
  l = fmaf(l, al, pp);                                                 \
  const float al0 = __builtin_bit_cast(float, __builtin_amdgcn_readlane(__builtin_bit_cast(int, al), 0));  \
  const float al1 = __builtin_bit_cast(float, __builtin_amdgcn_readlane(__builtin_bit_cast(int, al), 16)); \
  _Pragma("unroll") for (int k = 0; k < 8; ++k) { acc[0][k] *= al0; acc[1][k] *= al1; } \
  const unsigned ppk = cvtpk(pp, dppf<XOR1>(pp));     \
  const auto ph = __builtin_amdgcn_permlane16_swap(ppk, ppk, false, false);
#define PV_STEP(t)                                                     \
  {                                                                    \
    const unsigned p0 = dppu<BCAST + 2 * (t)>(ph[0]), p1 = dppu<BCAST + 2 * (t)>(ph[1]); \
    _Pragma("unroll") for (int k = 0; k < 4; ++k) {                    \
      const unsigned lo = pk_lo(V[2 * (t)][k], V[2 * (t) + 1][k]), hi = pk_hi(V[2 * (t)][k], V[2 * (t) + 1][k]); \
      acc[0][2 * k] = dot2(lo, p0, acc[0][2 * k]); acc[0][2 * k + 1] = dot2(hi, p0, acc[0][2 * k + 1]); \
      acc[1][2 * k] = dot2(lo, p1, acc[1][2 * k]); acc[1][2 * k + 1] = dot2(hi, p1, acc[1][2 * k + 1]); \
    }                                                                  \
    V[2 * (t)] = LDV(2 * (t), nxt); V[2 * (t) + 1] = LDV(2 * (t) + 1, nxt); \
    SB();                                                              \
  }
#define PV_ALL() PV_STEP(0) PV_STEP(1) PV_STEP(2) PV_STEP(3) PV_STEP(4) PV_STEP(5) PV_STEP(6) PV_STEP(7)

  {
    float y[16];
    const int nxt = 4 + sp;
#pragma unroll
    for (int i = 0; i < 16; ++i) {
      QK_STEP(KX, i)
      SB();
      ISSUE(H_C + 2 + i / H_D, (i + 1) % H_D == 0 ? H_C + 2 + (i + 1) / H_D : 0)
    }
    STAMP(17);
    SOFTMAX()
    SB();
    STAMP(18);
    ISSUE(H_C + 2 + 16 / H_D, 48)
    PV_ALL()
    STAMP(19);
  }
  v4u BW[2][4];
#pragma unroll
  for (int c = 0; c < 2; ++c)
#pragma unroll
    for (int ks = 0; ks < 4; ++ks) {
      LAS unsigned char* a = L + L_WO + (32 * ks + 8 * (lane >> 4) + ((lane >> 2) & 3)) * 256 + (2 * w + c) * 32 + (lane & 3) * 8;
      const v2u r0 = __builtin_bit_cast(v2u, __builtin_amdgcn_ds_read_tr16_b64_v4i16((LAS v4s*)a));
      const v2u r1 = __builtin_bit_cast(v2u, __builtin_amdgcn_ds_read_tr16_b64_v4i16((LAS v4s*)(a + 1024)));
      BW[c][ks] = v4u{r0.x, r0.y, r1.x, r1.y};
    }
  STAMP(5);
  SB();
  {
    volatile LAS unsigned* tks = (volatile LAS unsigned*)(L + L_TK);
    const int aoff = tid == 0 ? 0 : OOB;
    unsigned n = 1, nxt;
#pragma unroll 1
    do {
      nxt = (n + 1) * 4 + sp;
      if (n + 1 >= NSTAT) {
        unsigned t = 0;
#pragma unroll 1
        for (unsigned spins = 0; spins < SPIN_CAP; ++spins) {
          t = __builtin_amdgcn_readfirstlane(tks[n + 1]);
          if (t) break;
        }
        nxt = min(t - 1u, (unsigned)NST) + 4 * NSTAT;
        if (n >= 40) nxt = NST;
      }
      const bool req = n + 2 >= NSTAT && nxt < NST;
      const unsigned tk = __builtin_amdgcn_raw_ptr_buffer_atomic_add_i32(1, rws, req ? aoff : OOB, cnt_off, AUX_SC1);
      SB();
      float y[16];
#pragma unroll
      for (int i = 0; i < 16; ++i) {
        QK_STEP(K, i)
        K[i] = LDK(i, nxt);
        SB();
      }
      SOFTMAX()
      SB();
      PV_ALL()
      tks[(req && w == 0) ? n + 2 : 63] = __builtin_amdgcn_readfirstlane(tk) + 1u;
      SB();
      ++n;
    } while (nxt < NST);
  }
  STAMP(6);

  {
    float o[8];
#pragma unroll
    for (int k = 0; k < 8; ++k) o[k] = sum32(acc[0][k], acc[1][k]);
    LAS f32x4* mp = (LAS f32x4*)(L + L_MRG + w * 2048 + lane * 32);
    mp[0] = f32x4{o[0], o[1], o[2], o[3]};
    mp[1] = f32x4{o[4], o[5], o[6], o[7]};
    l += dppf<ROR8>(l); l += dppf<ROR4>(l); l += dppf<ROR2>(l); l += dppf<ROR1>(l);
    l = sum32(l, l);
    *(LAS f32x2*)(L + L_ML + w * 16 + ((lane >> 4) & 1) * 8) = f32x2{m * SCL, l};
  }
  __syncthreads();
  {
    f32x2 ml[4], ov[4];
#pragma unroll
    for (int i = 0; i < 4; ++i) {
      ml[i] = *(const LAS f32x2*)(L + L_ML + i * 16 + (w >> 1) * 8);
      ov[i] = *(const LAS f32x2*)(L + L_MRG + i * 2048 + tid * 8);
    }
    const float M = __uint_as_float(__float_as_uint(fmaxf(fmaxf(ml[0].x, ml[1].x), fmaxf(ml[2].x, ml[3].x))) & 0xffffff00u);
    float o0 = 0.f, o1 = 0.f, ls = 0.f;
#pragma unroll
    for (int i = 0; i < 4; ++i) {
      const float e = __builtin_amdgcn_exp2f(ml[i].x - M);
      o0 = fmaf(e, ov[i].x, o0); o1 = fmaf(e, ov[i].y, o1); ls = fmaf(e, ml[i].y, ls);
    }
    __builtin_amdgcn_raw_buffer_store_b128(v4u{enc_f(o0, taglo), taghi, enc_f(o1, taglo), taghi}, rws, tid * 16, WS_OG + bid * 4096, AUX_SC1);
    __builtin_amdgcn_raw_buffer_store_b128(v4u{enc_f(M, taglo), taghi, enc_f(ls, taglo), taghi}, rws, (tid & 127) == 0 ? 0 : OOB,
                                           WS_ML + (bid >> 2) * 128 + (w >> 1) * 64 + sp * 16, AUX_SC1);
  }
  STAMP(7);

  {
    v4u g[4], gm;
    const int og_off = WS_OG + (grp * 16 + w * 4) * 4096 + rb * 1024, ml_off = WS_ML + (grp * 4 + w) * 128 + (rb >> 1) * 64;
#pragma unroll 1
    for (unsigned spins = 0; spins < SPIN_CAP; ++spins) {
      MEMFENCE();
      gm = __builtin_amdgcn_raw_buffer_load_b128(rws, (lane & 3) * 16, ml_off, AUX_SC1);
#pragma unroll
      for (int s = 0; s < 4; ++s) g[s] = __builtin_amdgcn_raw_buffer_load_b128(rws, voff, og_off + s * 4096, AUX_SC1);
      if (!__any(bad2(gm, taglo, taghi) | bad2(g[0], taglo, taghi) | bad2(g[1], taglo, taghi) | bad2(g[2], taglo, taghi) | bad2(g[3], taglo, taghi))) break;
      __builtin_amdgcn_s_sleep(4);
    }
    const float Ms = dec_f(gm.x), Ls = dec_f(gm.z);
    float Mx = fmaxf(Ms, dppf<XOR1>(Ms));
    Mx = fmaxf(Mx, dppf<XOR2>(Mx));
    const float e = __builtin_amdgcn_exp2f(Ms - Mx);
    float Lt = e * Ls;
    Lt += dppf<XOR1>(Lt);
    Lt += dppf<XOR2>(Lt);
    const float c = e * __builtin_amdgcn_rcpf(Lt);
    const float c0 = dppf<0x00>(c), c1 = dppf<0x55>(c), c2 = dppf<0xAA>(c), c3 = dppf<0xFF>(c);
    const float o0 = fmaf(c0, dec_f(g[0].x), c1 * dec_f(g[1].x)) + fmaf(c2, dec_f(g[2].x), c3 * dec_f(g[3].x));
    const float o1 = fmaf(c0, dec_f(g[0].z), c1 * dec_f(g[1].z)) + fmaf(c2, dec_f(g[2].z), c3 * dec_f(g[3].z));
    *(LAS unsigned*)(L + L_O + w * 256 + lane * 4) = cvtpk(o0, o1);
  }
  STAMP(8);
  __syncthreads();
  {
    f32x4 d0 = {0.f, 0.f, 0.f, 0.f}, d1 = {0.f, 0.f, 0.f, 0.f};
#pragma unroll
    for (int ks = 0; ks < 4; ++ks) {
      const v4u a = *(const LAS v4u*)(L + L_O + ((lane & 15) >> 2) * 256 + ks * 64 + (lane >> 4) * 16);
      d0 = __builtin_amdgcn_mfma_f32_16x16x32_bf16(__builtin_bit_cast(bf16x8, a), __builtin_bit_cast(bf16x8, BW[0][ks]), d0, 0, 0, 0);
      d1 = __builtin_amdgcn_mfma_f32_16x16x32_bf16(__builtin_bit_cast(bf16x8, a), __builtin_bit_cast(bf16x8, BW[1][ks]), d1, 0, 0, 0);
    }
    const int po = (lane >> 4) * (28 * 4096) + (lane & 15) * 8;
    const int pg_off = WS_PG + (br * 4 * 28 + cb * 7 + 2 * w) * 4096 + (kvh * 4 + rb) * 128;
    __builtin_amdgcn_raw_buffer_store_b64(v2u{enc_f(d0[0], taglo), taghi}, rws, po, pg_off, AUX_SC1);
    __builtin_amdgcn_raw_buffer_store_b64(v2u{enc_f(d1[0], taglo), taghi}, rws, w < 3 ? po : OOB, pg_off + 4096, AUX_SC1);
  }
  STAMP(9);

  const int ri = bid & 127;
  if (w == 3 && ri < 112) {
    v4u g[4];
    const int pg_off = WS_PG + (br * 112 + ri) * 4096;
#pragma unroll 1
    for (unsigned spins = 0; spins < SPIN_CAP; ++spins) {
      MEMFENCE();
#pragma unroll
      for (int s = 0; s < 4; ++s) g[s] = __builtin_amdgcn_raw_buffer_load_b128(rws, voff + s * 1024, pg_off, AUX_SC1);
      if (!__any(bad2(g[0], taglo, taghi) | bad2(g[1], taglo, taghi) | bad2(g[2], taglo, taghi) | bad2(g[3], taglo, taghi))) break;
      __builtin_amdgcn_s_sleep(2);
    }
    float s0 = (dec_f(g[0].x) + dec_f(g[1].x)) + (dec_f(g[2].x) + dec_f(g[3].x));
    float s1 = (dec_f(g[0].z) + dec_f(g[1].z)) + (dec_f(g[2].z) + dec_f(g[3].z));
    s0 += dppf<ROR8>(s0); s1 += dppf<ROR8>(s1);
    s0 = sum16(s0, s0); s1 = sum16(s1, s1);
    s0 = sum32(s0, s0); s1 = sum32(s1, s1);
    const auto ro = __builtin_amdgcn_make_buffer_rsrc(p.out, 0, 4 * 2 * DM * 2, RSRC_W3);
    const int ob = ri / 28, ct = ri - ob * 28;
    __builtin_amdgcn_raw_buffer_store_b32(cvtpk(s0, s1), ro, lane < 8 ? lane * 4 : OOB, (ob * 2 * DM + br * DM + ct * 16) * 2, 0);
    __builtin_amdgcn_raw_buffer_store_b64(v2u{(unsigned)tag, (unsigned)(tag >> 32)}, rws, (ri == 0 && lane == 0) ? 0 : OOB, WS_EPOCH + br * 256, AUX_SC1);
  }
}

}

extern "C" void kernel_launch(void* const* d_in, const int* in_sizes, int n_in, void* d_out, int out_size, void* d_ws, size_t ws_size,
                              hipStream_t stream) {
  Params p;
  for (int i = 0; i < 2; ++i) {
    p.x[i] = d_in[5 * i + 0];
    p.k[i] = d_in[5 * i + 1];
    p.v[i] = d_in[5 * i + 2];
    p.wq[i] = d_in[5 * i + 3];
    p.wo[i] = d_in[5 * i + 4];
  }
  for (int i = 0; i < 10; ++i) fprintf(stderr, "in[%d]=%p ", i, d_in[i]);
  fprintf(stderr, "out=%p ws=%p\n", d_out, d_ws);
  p.out = d_out;
  p.ws = (unsigned char*)d_ws;
  decode_fused<<<256, 256, 0, stream>>>(p);
}
```

```cpp
#define LIGHT
#include <hip/hip_runtime.h>
#include <hip/hip_bf16.h>
#include <stdint.h>

namespace {

typedef unsigned v4u __attribute__((ext_vector_type(4)));
typedef unsigned v2u __attribute__((ext_vector_type(2)));
typedef float f32x4 __attribute__((ext_vector_type(4)));
typedef float f32x2 __attribute__((ext_vector_type(2)));
typedef short v4s __attribute__((ext_vector_type(4)));
typedef __bf16 bf16x2 __attribute__((ext_vector_type(2)));
typedef __bf16 bf16x8 __attribute__((ext_vector_type(8)));
#define LAS __attribute__((address_space(3)))
#define SB() __builtin_amdgcn_sched_barrier(0)
#define MEMFENCE() asm volatile("" ::: "memory")

constexpr int DM = 448, NQ = 4096, SK = 8192, DH = 256;
constexpr unsigned REGION_BYTES = SK * DH * 2;
#define PACE_GBS 7600
#define PACE_OFF 220
#define PACE_STAG 4
#define PACE_TAIL 8
constexpr int PACE_PER = (int)(1048576.0 * 256.0 / (PACE_GBS * 10.0) + 0.5);
#define SLEEP_A 4
#define SLEEP_B 2
#define AUX_QST 16
#define AUX_QLD 16
#define DBG_PUB 32
#define DBG_POLL qg_v
constexpr int AUX_NT = 2, AUX_SC1 = 16, RSRC_W3 = 0x00020000;
constexpr int OOB = 0x7ffffff0;
constexpr unsigned SPIN_CAP = 1u << 18;
constexpr float SCL = 0.0625f * 1.4426950408889634f;

constexpr unsigned WS_EPOCH = 0;
constexpr unsigned WS_ML = 16384;
constexpr unsigned WS_QG = 32768;
constexpr unsigned WS_OG = WS_QG + 256 * 512;
constexpr unsigned WS_PG = WS_OG + 2 * 256 * 2048;
constexpr unsigned WS_END = WS_PG + 128 * 4096;

constexpr int L_WO = 0;
constexpr int L_MRG = 32768;
constexpr int L_QP = L_MRG + 16384;
constexpr int L_Q = L_QP + 2048;
constexpr int L_O = L_Q + 2048;
constexpr int L_ML = L_O + 1024;
constexpr int L_SC = L_ML + 128;
constexpr int L_BYTES = 84 * 1024;
static_assert(L_SC + 8 * 1024 <= L_BYTES && L_SC % 16 == 0, "lds map");

struct Params {
  const void *x0, *k0, *v0, *wq0, *wo0, *x1, *k1, *v1, *wq1, *wo1;
  void* out;
  unsigned char* ws;
#define STBASE p.ws
};

__device__ __forceinline__ float dot2(unsigned a, unsigned b, float c) {
  return __builtin_amdgcn_fdot2_f32_bf16(__builtin_bit_cast(bf16x2, a), __builtin_bit_cast(bf16x2, b), c, false);
}
__device__ __forceinline__ unsigned pk_lo(unsigned a, unsigned b) { return __builtin_amdgcn_perm(b, a, 0x05040100u); }
__device__ __forceinline__ unsigned pk_hi(unsigned a, unsigned b) { return __builtin_amdgcn_perm(b, a, 0x07060302u); }
__device__ __forceinline__ unsigned cvtpk(float lo, float hi) {
  bf16x2 c = {(__bf16)lo, (__bf16)hi};
  return __builtin_bit_cast(unsigned, c);
}
template <int CTRL> __device__ __forceinline__ float dppf(float x) {
  return __builtin_bit_cast(float, __builtin_amdgcn_mov_dpp(__builtin_bit_cast(int, x), CTRL, 0xf, 0xf, true));
}
template <int CTRL> __device__ __forceinline__ unsigned dppu(unsigned x) {
  return (unsigned)__builtin_amdgcn_mov_dpp((int)x, CTRL, 0xf, 0xf, true);
}
constexpr int XOR1 = 0xB1, XOR2 = 0x4E, XOR7 = 0x141, ROR8 = 0x128, ROR4 = 0x124, ROR2 = 0x122, ROR1 = 0x121, BCAST = 0x150;
__device__ __forceinline__ float sum16(float a, float b) {
  auto r = __builtin_amdgcn_permlane16_swap(__float_as_uint(a), __float_as_uint(b), false, false);
  return __uint_as_float(r[0]) + __uint_as_float(r[1]);
}
__device__ __forceinline__ float sum32(float a, float b) {
  auto r = __builtin_amdgcn_permlane32_swap(__float_as_uint(a), __float_as_uint(b), false, false);
  return __uint_as_float(r[0]) + __uint_as_float(r[1]);
}
__device__ __forceinline__ float max32(float a) {
  auto r = __builtin_amdgcn_permlane32_swap(__float_as_uint(a), __float_as_uint(a), false, false);
  return fmaxf(__uint_as_float(r[0]), __uint_as_float(r[1]));
}
__device__ __forceinline__ unsigned enc_f(float f, unsigned tag) { return __builtin_amdgcn_alignbit(tag, __float_as_uint(f), 8); }
__device__ __forceinline__ float dec_f(unsigned d) { return __uint_as_float(d << 8); }
__device__ __forceinline__ unsigned diff4(v4u g, unsigned t) { return ((g.x ^ t) | (g.y ^ t)) | ((g.z ^ t) | (g.w ^ t)); }

#define STAMP(i) do { } while (0)
#define TRACE(units) do { } while (0)

__global__ void __launch_bounds__(256, 1) decode_fused(Params p) {
  __shared__ __attribute__((aligned(16))) unsigned char smem[L_BYTES];
  LAS unsigned char* L = (LAS unsigned char*)smem;
  const unsigned t0 = (unsigned)__builtin_amdgcn_s_memrealtime();
  int probe = 0;
  const int bid = blockIdx.x, kvh = bid >> 5, b = (bid >> 3) & 3, sp = bid & 7;
  const int br = (bid >> 4) & 1, j = bid & 15;
  const int tid = threadIdx.x, lane = tid & 63, w = __builtin_amdgcn_readfirstlane(tid >> 6);
  const int qs = (j & 7) * 2 + (j >> 3);
  const int cb = (j & 1) * 2 + (j >> 3), rb = (j >> 1) & 3;

  unsigned cpf;
  {
    unsigned long long pc;
    unsigned len;
    asm volatile("s_getpc_b64 %0\n.Ldec_code_begin:\n\ts_mov_b32 %1, .Ldec_code_end-.Ldec_code_begin" : "=s"(pc), "=s"(len));
    const auto rc = __builtin_amdgcn_make_buffer_rsrc((void*)pc, 0, len, RSRC_W3);
    cpf = __builtin_amdgcn_raw_buffer_load_b32(rc, tid < 8 ? tid * 128 : OOB, (bid >> 3) * 1024, 0);
    SB();
  }
  const auto rws = __builtin_amdgcn_make_buffer_rsrc(p.ws, 0, WS_END, RSRC_W3);
  const auto rx = __builtin_amdgcn_make_buffer_rsrc((void*)(br ? p.x1 : p.x0), 0, 4 * DM * 2, RSRC_W3);
  const auto rwq = __builtin_amdgcn_make_buffer_rsrc((void*)(br ? p.wq1 : p.wq0), 0, DM * NQ * 2, RSRC_W3);
  const auto rwo = __builtin_amdgcn_make_buffer_rsrc((void*)(br ? p.wo1 : p.wo0), 0, NQ * DM * 2, RSRC_W3);
  const size_t reg_off = (size_t)(b * 8 + kvh) * REGION_BYTES + sp * (REGION_BYTES / 8);

  const unsigned ep = __builtin_amdgcn_raw_buffer_load_b32(rws, 0, WS_EPOCH, AUX_SC1);
  v4u X[4], W[8], WO[8], X0[16], X1[16], R[16];
  {
    const int row0 = 128 * w + 8 * (lane >> 2);
    const int xoff = row0 < DM ? row0 * 2 : OOB;
#pragma unroll
    for (int i = 0; i < 4; ++i) X[i] = __builtin_amdgcn_raw_buffer_load_b128(rx, xoff, i * DM * 2, 0);
    const int woff = row0 * (NQ * 2) + (lane & 3) * 16;
#pragma unroll
    for (int n = 0; n < 8; ++n) W[n] = __builtin_amdgcn_raw_buffer_load_b128(rwq, woff, (kvh * 512 + qs * 32) * 2 + n * (NQ * 2), AUX_NT);
  }
  SB();
  {
    const int ooff = (lane & 15) < 14 ? (lane >> 4) * (DM * 2) + (lane & 15) * 16 : OOB;
#pragma unroll
    for (int n = 0; n < 8; ++n)
      WO[n] = __builtin_amdgcn_raw_buffer_load_b128(rwo, ooff, (kvh * 512 + rb * 128 + 32 * w + 4 * n) * (DM * 2) + cb * 224, AUX_NT);
  }
  SB();
  const int voff = lane * 16, koff = tid * 16;
#define LDU(rs, i) __builtin_amdgcn_raw_buffer_load_b128(rs, koff, (i) * 4096, AUX_NT)
  const int poff = PACE_OFF * 256 + ((bid * 157) & 255) * (PACE_STAG * PACE_PER / 256);
#define LATE(x, tgt)
#define PACE_BAR
#define PACE(x)                                                                           \
  {                                                                                       \
    const unsigned tgt_ = t0 + ((min((int)(x), 512 - PACE_TAIL) * PACE_PER + poff) >> 8); \
    LATE(x, tgt_)                                                                         \
    asm volatile("1:\n\ts_memrealtime vcc\n\ts_waitcnt lgkmcnt(0)\n\ts_sub_u32 vcc_lo, vcc_lo, %0\n\ts_cmp_lt_i32 vcc_lo, 0\n\t" \
                 "s_cbranch_scc0 2f\n\ts_sleep 1\n\ts_branch 1b\n2:" PACE_BAR :: "s"(tgt_) : "vcc", "scc", "memory"); \
    SB();                                                                                 \
  }
  const auto rk0 = __builtin_amdgcn_make_buffer_rsrc((char*)p.k0 + reg_off, 0, 48 * 4096, RSRC_W3);
#define ISSUE(lo, hi)                                                                     \
  _Pragma("unroll") for (int i_ = (lo); i_ < (hi) && i_ < 48; ++i_) {                    \
    if (i_ >= 4 && (i_ & 3) == 0) PACE(i_)                                                \
    if (i_ < 16) X0[i_ & 15] = LDU(rk0, i_);                                              \
    else if (i_ < 32) X1[i_ & 15] = LDU(rk0, i_);                                         \
    else R[i_ & 15] = LDU(rk0, i_);                                                       \
    SB();                                                                                 \
  }                                                                                       \
  SB();
#define H_P 16
#define H_C 28
#define H_Q (H_C < 24 ? 24 : H_C)
  STAMP(12);

  {
    float a[4][8];
#pragma unroll
    for (int bb = 0; bb < 4; ++bb)
#pragma unroll
      for (int c = 0; c < 8; ++c) a[bb][c] = 0.f;
#pragma unroll
    for (int t = 0; t < 4; ++t) {
#pragma unroll
      for (int k = 0; k < 4; ++k) {
        const unsigned lo = pk_lo(W[2 * t][k], W[2 * t + 1][k]), hi = pk_hi(W[2 * t][k], W[2 * t + 1][k]);
#pragma unroll
        for (int bb = 0; bb < 4; ++bb) {
          a[bb][2 * k] = dot2(lo, X[bb][t], a[bb][2 * k]);
          a[bb][2 * k + 1] = dot2(hi, X[bb][t], a[bb][2 * k + 1]);
        }
      }
      SB();
      ISSUE(t, t + 1)
    }
    asm volatile("" :: "v"(cpf));
    STAMP(14);
    float y[8];
#pragma unroll
    for (int c = 0; c < 8; ++c) {
      y[c] = sum16(sum32(a[0][c], a[2][c]), sum32(a[1][c], a[3][c]));
      y[c] += dppf<ROR8>(y[c]);
      y[c] += dppf<ROR4>(y[c]);
    }
    LAS f32x4* qp = (LAS f32x4*)(L + L_QP + w * 512 + (lane >> 4) * 128 + (lane & 3) * 32);
    qp[0] = f32x4{y[0], y[1], y[2], y[3]};
    qp[1] = f32x4{y[4], y[5], y[6], y[7]};
  }
  STAMP(1);
  __syncthreads();
  const unsigned tag = __builtin_amdgcn_readfirstlane(ep) + 1u, tag24 = tag << 24, tag16 = tag << 16;
  if (w == 0) {
    const LAS f32x4* qp = (const LAS f32x4*)(L + L_QP + (lane & 31) * 16);
    const f32x4 q = (qp[0] + qp[32]) + (qp[64] + qp[96]);
    const unsigned lo = cvtpk(q.x, q.y), hi = cvtpk(q.z, q.w);
    __builtin_amdgcn_raw_buffer_store_b128(v4u{pk_lo(lo, tag), __builtin_amdgcn_alignbit(tag, lo, 16), pk_lo(hi, tag), __builtin_amdgcn_alignbit(tag, hi, 16)}, rws,
                                           lane < DBG_PUB ? lane * 16 : OOB, WS_QG + (kvh * 32 + br * 16 + qs) * 512, AUX_QST);
  }
  SB();
  STAMP(2);
  ISSUE(4, H_P)
  const int qg_off = WS_QG + kvh * 32 * 512 + b * 128, qg_v = (tid >> 3) * 512 + (tid & 7) * 16;
  v4u qg = __builtin_amdgcn_raw_buffer_load_b128(rws, DBG_POLL, qg_off, AUX_QLD);
  SB();
  STAMP(3);
#pragma unroll
  for (int n = 0; n < 8; ++n) *(LAS v4u*)(L + L_WO + (32 * w + 4 * n) * 256 + lane * 16) = WO[n];
  SB();
  ISSUE(H_P, H_C)
  STAMP(15);
  if (DBG_PUB && __any(diff4(qg, tag16) >> 16)) {
#pragma unroll 1
    for (unsigned spins = 0; spins < SPIN_CAP; ++spins) {
      MEMFENCE();
      qg = __builtin_amdgcn_raw_buffer_load_b128(rws, qg_v, qg_off, AUX_QLD);
      if (!__any(diff4(qg, tag16) >> 16)) break;
      __builtin_amdgcn_s_sleep(2);
    }
  }
  *(LAS v2u*)(L + L_Q + tid * 8) = v2u{pk_lo(qg.x, qg.y), pk_lo(qg.z, qg.w)};
  STAMP(4);
  SB();
  __syncthreads();
  v4u q0 = *(const LAS v4u*)(L + L_Q + (lane & 31) * 16), q1 = *(const LAS v4u*)(L + L_Q + 512 + (lane & 31) * 16);

  float acc[2][8], m = -INFINITY, l = 0.f;
#pragma unroll
  for (int k = 0; k < 8; ++k) acc[0][k] = acc[1][k] = 0.f;
  const bool b3 = lane & 8, b2 = lane & 4, b1 = lane & 2, b0 = lane & 1;
  LAS float* const sc = (LAS float*)(L + L_SC + tid * 4);

#define QK_STEP(KR, i)                                                 \
  {                                                                    \
    float s0 = dot2(KR[i][0], q0[0], 0.f), s1 = dot2(KR[i][0], q1[0], 0.f); \
    s0 = dot2(KR[i][1], q0[1], s0); s1 = dot2(KR[i][1], q1[1], s1);    \
    s0 = dot2(KR[i][2], q0[2], s0); s1 = dot2(KR[i][2], q1[2], s1);    \
    s0 = dot2(KR[i][3], q0[3], s0); s1 = dot2(KR[i][3], q1[3], s1);    \
    y[i] = sum16(s0, s1);                                              \
  }
#define TR_STEP(H, CTRL, BIT)                                          \
  _Pragma("unroll") for (int i = 0; i < (H); ++i) {                    \
    const float keep = (BIT) ? y[i + (H)] : y[i], send = (BIT) ? y[i] : y[i + (H)]; \
    y[i] = keep + dppf<CTRL>(send);                                    \
  }
#define SCORES(slot)                                                   \
  TR_STEP(8, ROR8, b3) TR_STEP(4, XOR7, b2) TR_STEP(2, XOR2, b1) TR_STEP(1, XOR1, b0) \
  m = fmaxf(m, y[0]);                                                  \
  sc[(slot) * 256] = y[0];
#define PV_STEP(t)                                                     \
  {                                                                    \
    const unsigned p0 = dppu<BCAST + 2 * (t)>(ph[0]), p1 = dppu<BCAST + 2 * (t)>(ph[1]); \
    _Pragma("unroll") for (int k = 0; k < 4; ++k) {                    \
      const unsigned lo = pk_lo(R[2 * (t)][k], R[2 * (t) + 1][k]), hi = pk_hi(R[2 * (t)][k], R[2 * (t) + 1][k]); \
      acc[0][2 * k] = dot2(lo, p0, acc[0][2 * k]); acc[0][2 * k + 1] = dot2(hi, p0, acc[0][2 * k + 1]); \
      acc[1][2 * k] = dot2(lo, p1, acc[1][2 * k]); acc[1][2 * k + 1] = dot2(hi, p1, acc[1][2 * k + 1]); \
    }                                                                  \
  }
#define PROBS(slot)                                                                     \
  const float pp = __builtin_amdgcn_exp2f((sc[(slot) * 256] - m) * SCL);                \
  l += pp;                                                                              \
  const unsigned ppk = cvtpk(pp, dppf<XOR1>(pp));        \
  const auto ph = __builtin_amdgcn_permlane16_swap(ppk, ppk, false, false);
#define BURST(g)                                                       \
  SB();                                                                \
  PACE(x + 4 * (g))                                                    \
  _Pragma("unroll") for (int u_ = 4 * (g); u_ < 4 * (g) + 4; ++u_) { R[u_] = LDU(rn, u_); SB(); }
#define DUMP(brn)                                                      \
  {                                                                    \
    float o[8];                                                        \
    _Pragma("unroll") for (int k = 0; k < 8; ++k) o[k] = sum32(acc[0][k], acc[1][k]);     \
    LAS f32x4* mp = (LAS f32x4*)(L + L_MRG + (brn) * 8192 + tid * 32); \
    mp[0] = f32x4{o[0], o[1], o[2], o[3]};                             \
    mp[1] = f32x4{o[4], o[5], o[6], o[7]};                             \
    l += dppf<ROR8>(l); l += dppf<ROR4>(l); l += dppf<ROR2>(l); l += dppf<ROR1>(l); \
    l = sum32(l, l);                                                   \
    *(LAS f32x2*)(L + L_ML + (brn) * 64 + w * 16 + ((lane >> 4) & 1) * 8) = f32x2{m * SCL, l}; \
  }

  {
    float y[16];
    ISSUE(H_C, H_Q)
#pragma unroll
    for (int i = 0; i < 16; ++i) {
      if ((i & 7) == 0) { SB(); ISSUE(H_Q + (i >> 3) * 4, H_Q + (i >> 3) * 4 + 4) }
      QK_STEP(X0, i)
    }
    SCORES(0)
    STAMP(17);
#pragma unroll
    for (int i = 0; i < 16; ++i) {
      if ((i & 7) == 0) { SB(); ISSUE(H_Q + 8 + (i >> 3) * 4, H_Q + 8 + (i >> 3) * 4 + 4) }
      QK_STEP(X1, i)
    }
    SCORES(1)
    SB();
    STAMP(18);
  }
  v4u BW[2][4];
#pragma unroll
  for (int c = 0; c < 2; ++c)
#pragma unroll
    for (int ks = 0; ks < 4; ++ks) {
      LAS unsigned char* a = L + L_WO + (32 * ks + 8 * (lane >> 4) + ((lane >> 2) & 3)) * 256 + (2 * w + c) * 32 + (lane & 3) * 8;
      const v2u r0 = __builtin_bit_cast(v2u, __builtin_amdgcn_ds_read_tr16_b64_v4i16((LAS v4s*)a));
      const v2u r1 = __builtin_bit_cast(v2u, __builtin_amdgcn_ds_read_tr16_b64_v4i16((LAS v4s*)(a + 1024)));
      BW[c][ks] = v4u{r0.x, r0.y, r1.x, r1.y};
    }
  SB();
  ISSUE(H_Q + 16, 48)
  STAMP(5);
  TRACE(16); TRACE(16);
  {
    const char *kp = (const char*)p.k0 + reg_off, *vp = (const char*)p.v0 + reg_off;
    int x = 48, g = 2;
#pragma unroll 1
    for (int grp = 0;; ++grp) {
#pragma unroll 1
      do {
        const auto rn = __builtin_amdgcn_make_buffer_rsrc((void*)(g < 7 ? kp + (g + 1) * 65536 : vp), 0, 65536, RSRC_W3);
        float y[16];
#pragma unroll
        for (int i = 0; i < 16; ++i) {
          QK_STEP(R, i)
          if ((i & 3) == 3) { BURST(i >> 2) }
        }
        SCORES(g)
        SB();
        TRACE(16);
        x += 16;
      } while (++g < 8);
      m = fmaxf(m, dppf<ROR8>(m)); m = fmaxf(m, dppf<ROR4>(m)); m = fmaxf(m, dppf<ROR2>(m)); m = fmaxf(m, dppf<ROR1>(m));
      m = max32(m);
      kp = (const char*)p.k1 + reg_off;
      g = 0;
#pragma unroll 1
      do {
        const auto rn = __builtin_amdgcn_make_buffer_rsrc((void*)(g < 7 ? vp + (g + 1) * 65536 : kp), 0, 65536, RSRC_W3);
        PROBS(g)
        PV_STEP(0) PV_STEP(1) BURST(0) PV_STEP(2) PV_STEP(3) BURST(1) PV_STEP(4) PV_STEP(5) BURST(2) PV_STEP(6) PV_STEP(7) BURST(3)
        TRACE(16);
        x += 16;
      } while (++g < 8 - grp);
      if (grp) break;
      DUMP(0)
#pragma unroll
      for (int k = 0; k < 8; ++k) acc[0][k] = acc[1][k] = 0.f;
      m = -INFINITY; l = 0.f;
      q0 = *(const LAS v4u*)(L + L_Q + 1024 + (lane & 31) * 16);
      q1 = *(const LAS v4u*)(L + L_Q + 1536 + (lane & 31) * 16);
      vp = (const char*)p.v1 + reg_off;
      g = 0;
    }
    STAMP(19);
    {
      PROBS(7)
      PV_STEP(0) PV_STEP(1) PV_STEP(2) PV_STEP(3) PV_STEP(4) PV_STEP(5) PV_STEP(6) PV_STEP(7)
      TRACE(16);
    }
  }
  STAMP(6);

  DUMP(1)
  __syncthreads();
  {
    f32x2 ml[4];
    f32x4 ov[4];
#pragma unroll
    for (int i = 0; i < 4; ++i) {
      ml[i] = *(const LAS f32x2*)(L + L_ML + (w >> 1) * 64 + i * 16 + (w & 1) * 8);
      ov[i] = *(const LAS f32x4*)(L + L_MRG + (w >> 1) * 8192 + i * 2048 + (w & 1) * 1024 + lane * 16);
    }
    const float M = __uint_as_float(__float_as_uint(fmaxf(fmaxf(ml[0].x, ml[1].x), fmaxf(ml[2].x, ml[3].x))) & 0xffffff00u);
    f32x4 o = {0.f, 0.f, 0.f, 0.f};
    float ls = 0.f;
#pragma unroll
    for (int i = 0; i < 4; ++i) {
      const float e = __builtin_amdgcn_exp2f(ml[i].x - M);
      o += e * ov[i];
      ls = fmaf(e, ml[i].y, ls);
    }
    __builtin_amdgcn_raw_buffer_store_b128(v4u{enc_f(o.x, tag), enc_f(o.y, tag), enc_f(o.z, tag), enc_f(o.w, tag)}, rws, voff,
                                           WS_OG + ((w >> 1) * 256 + bid) * 2048 + (w & 1) * 1024, AUX_SC1);
    __builtin_amdgcn_raw_buffer_store_b128(v4u{enc_f(M, tag), tag, enc_f(ls, tag), tag}, rws, lane == 0 ? 0 : OOB,
                                           WS_ML + ((w >> 1) * 32 + (bid >> 3)) * 256 + (w & 1) * 128 + sp * 16, AUX_SC1);
  }
  STAMP(7);

  {
    v4u g[4], gm;
    const int og_off = WS_OG + (br * 256 + kvh * 32 + w * 8) * 2048 + rb * 512, og_v = (lane >> 5) * 2048 + (lane & 31) * 16;
    const int ml_off = WS_ML + (br * 32 + kvh * 4 + w) * 256 + (rb >> 1) * 128, ml_v = (((lane & 3) * 2 + (lane >> 5)) ^ ((lane >> 2) & 1)) * 16;
#define ML_BAD() ((((gm.x ^ tag24) | (gm.z ^ tag24)) >> 24) | (gm.y ^ tag) | (gm.w ^ tag))
#pragma unroll 1
    for (unsigned spins = 0; spins < SPIN_CAP; ++spins) {
      MEMFENCE();
      gm = __builtin_amdgcn_raw_buffer_load_b128(rws, ml_v, ml_off, AUX_SC1);
      if (!__any(ML_BAD())) break;
      __builtin_amdgcn_s_sleep(1);
    }
#pragma unroll 1
    for (unsigned spins = 0; spins < SPIN_CAP; ++spins) {
      MEMFENCE();
#pragma unroll
      for (int s = 0; s < 4; ++s) g[s] = __builtin_amdgcn_raw_buffer_load_b128(rws, og_v, og_off + s * 4096, AUX_SC1);
      if (!__any(ML_BAD() | ((diff4(g[0], tag24) | diff4(g[1], tag24) | diff4(g[2], tag24) | diff4(g[3], tag24)) >> 24))) break;
      __builtin_amdgcn_s_sleep(SLEEP_A);
    }
    const float Ms = dec_f(gm.x), Ls = dec_f(gm.z);
    float Mx = fmaxf(Ms, dppf<XOR1>(Ms));
    Mx = fmaxf(Mx, dppf<XOR2>(Mx));
    Mx = fmaxf(Mx, dppf<XOR7>(Mx));
    const float e = __builtin_amdgcn_exp2f(Ms - Mx);
    float Lt = e * Ls;
    Lt += dppf<XOR1>(Lt);
    Lt += dppf<XOR2>(Lt);
    Lt += dppf<XOR7>(Lt);
    const float c = e * __builtin_amdgcn_rcpf(Lt);
    float o0 = 0.f, o1 = 0.f, o2 = 0.f, o3 = 0.f;
#define MRG(s) { const float cs = dppf<BCAST + s>(c); o0 = fmaf(cs, dec_f(g[s].x), o0); o1 = fmaf(cs, dec_f(g[s].y), o1); o2 = fmaf(cs, dec_f(g[s].z), o2); o3 = fmaf(cs, dec_f(g[s].w), o3); }
    MRG(0) MRG(1) MRG(2) MRG(3)
    *(LAS unsigned*)(L + L_O + w * 256 + (lane & 31) * 8 + (lane >> 5) * 4) = cvtpk(sum32(o0, o2), sum32(o1, o3));
  }
  STAMP(8);
  __syncthreads();
  {
    f32x4 d0 = {0.f, 0.f, 0.f, 0.f}, d1 = {0.f, 0.f, 0.f, 0.f};
#pragma unroll
    for (int ks = 0; ks < 4; ++ks) {
      const v4u o = *(const LAS v4u*)(L + L_O + (lane & 3) * 256 + ks * 64 + (lane >> 4) * 16);
      d0 = __builtin_amdgcn_mfma_f32_16x16x32_bf16(__builtin_bit_cast(bf16x8, BW[0][ks]), __builtin_bit_cast(bf16x8, o), d0, 0, 0, 0);
      d1 = __builtin_amdgcn_mfma_f32_16x16x32_bf16(__builtin_bit_cast(bf16x8, BW[1][ks]), __builtin_bit_cast(bf16x8, o), d1, 0, 0, 0);
    }
    const f32x4 d = b2 ? d1 : d0;
    __builtin_amdgcn_raw_buffer_store_b128(v4u{enc_f(d.x, tag), enc_f(d.y, tag), enc_f(d.z, tag), enc_f(d.w, tag)}, rws,
                                           b3 ? OOB : (lane & 3) * 4096 + (lane & 4) * 16 + (lane >> 4) * 16,
                                           WS_PG + ((br * 4 + cb) * 4 + w) * 16384 + (kvh * 4 + rb) * 128, AUX_SC1);
  }
  STAMP(9);

  if (w == 3 && (bid & 1) == 0) {
    const int ri = bid >> 1;
    v4u g[4];
#pragma unroll 1
    for (unsigned spins = 0; spins < SPIN_CAP; ++spins) {
      MEMFENCE();
#pragma unroll
      for (int s = 0; s < 4; ++s) g[s] = __builtin_amdgcn_raw_buffer_load_b128(rws, voff + s * 1024, WS_PG + ri * 4096, AUX_SC1);
      if (!__any((diff4(g[0], tag24) | diff4(g[1], tag24) | diff4(g[2], tag24) | diff4(g[3], tag24)) >> 24)) break;
      __builtin_amdgcn_s_sleep(SLEEP_B);
    }
    float sum[4];
#pragma unroll
    for (int k = 0; k < 4; ++k) {
      sum[k] = (dec_f(g[0][k]) + dec_f(g[1][k])) + (dec_f(g[2][k]) + dec_f(g[3][k]));
      sum[k] += dppf<ROR8>(sum[k]);
      sum[k] = sum16(sum[k], sum[k]);
      sum[k] = sum32(sum[k], sum[k]);
    }
    const auto ro = __builtin_amdgcn_make_buffer_rsrc(p.out, 0, 4 * 2 * DM * 2, RSRC_W3);
    __builtin_amdgcn_raw_buffer_store_b64(v2u{cvtpk(sum[0], sum[1]), cvtpk(sum[2], sum[3])}, ro, lane < ((ri & 12) == 12 ? 4 : 8) ? lane * 8 : OOB,
                                          ((ri & 3) * 2 * DM + (ri >> 6) * DM + ((ri >> 4) & 3) * 112 + ((ri >> 2) & 3) * 32) * 2, 0);
    __builtin_amdgcn_raw_buffer_store_b32(tag, rws, (bid == 0 && lane == 0) ? 0 : OOB, WS_EPOCH, AUX_SC1);
  }
  while ((true) && (int)((unsigned)__builtin_amdgcn_s_memrealtime() - t0) < 10000 + (0)) __builtin_amdgcn_s_sleep(1);
  asm volatile(".Ldec_code_end:");
}

}

extern "C" void kernel_launch(void* const* d_in, const int* in_sizes, int n_in, void* d_out, int out_size, void* d_ws, size_t ws_size,
                              hipStream_t stream) {
  Params p;
  p.x0 = d_in[0]; p.k0 = d_in[1]; p.v0 = d_in[2]; p.wq0 = d_in[3]; p.wo0 = d_in[4];
  p.x1 = d_in[5]; p.k1 = d_in[6]; p.v1 = d_in[7]; p.wq1 = d_in[8]; p.wo1 = d_in[9];
  p.out = d_out;
  p.ws = (unsigned char*)d_ws;
  decode_fused<<<256, 256, 0, stream>>>(p);
}
```

```cpp
#include <hip/hip_runtime.h>
#include <hip/hip_bf16.h>
#include <stdint.h>

namespace {

typedef unsigned v4u __attribute__((ext_vector_type(4)));
typedef unsigned v2u __attribute__((ext_vector_type(2)));
typedef float f32x4 __attribute__((ext_vector_type(4)));
typedef float f32x2 __attribute__((ext_vector_type(2)));
typedef short v4s __attribute__((ext_vector_type(4)));
typedef __bf16 bf16x2 __attribute__((ext_vector_type(2)));
typedef __bf16 bf16x8 __attribute__((ext_vector_type(8)));
#define LAS __attribute__((address_space(3)))
#define SB() __builtin_amdgcn_sched_barrier(0)
#define MEMFENCE() asm volatile("" ::: "memory")
#define DRAIN() asm volatile("s_waitcnt vmcnt(0)" ::: "memory")
#define LDS_BARRIER() asm volatile("s_waitcnt lgkmcnt(0)\n\ts_barrier" ::: "memory")
#define NOW() ((unsigned)__builtin_amdgcn_s_memrealtime())

constexpr int DM = 448, NQ = 4096, SK = 8192, DH = 256;
constexpr unsigned REGION_BYTES = SK * DH * 2;
#define PACE_GBS 7800
#define PACE_OFF 125
#define PACE_STAG 4
#define PACE_MUL 19
#define PACE_TAIL 8
#define PUB_NOM 235
#define H_A 8
#define H_P 28
#define H_C 36
#define H_S 14
#define EPMODE 2
#define KROT 4
#define QROT 0
#define H_I 0
#define SLIP_THR 70
#define SLIP_THR0 130
#define SLIP_N 5
#define EARLY 60
#define EARLY_STEP 5
#define WO_N 4
#define WO_AT 7
#define GAP_T 0
#define SLEEP_A 1
#define SLEEP_B 1
constexpr int PACE_PER = (int)(1048576.0 * 256.0 / (PACE_GBS * 10.0) + 0.5);
constexpr int AUX_NT = 2, AUX_SC1 = 16, RSRC_W3 = 0x00020000;
constexpr int OOB = 0x7ffffff0;
constexpr unsigned SPIN_CAP = 1u << 18;
constexpr float SCL = 0.0625f * 1.4426950408889634f;
static_assert(H_C >= 32 && H_C <= 44 && H_P <= H_C && H_P % 4 == 0 && H_C % 4 == 0, "head schedule");

constexpr unsigned WS_EPOCH = 0;
constexpr unsigned WS_ML = 16384;
constexpr unsigned WS_QG = 32768;
constexpr unsigned WS_OG = WS_QG + 256 * 512;
constexpr unsigned WS_PG = WS_OG + 2 * 256 * 2048;
constexpr unsigned WS_END = WS_PG + 128 * 4096;

constexpr int L_WO = 0;
constexpr int L_MRG = 32768;
constexpr int L_QP = L_MRG + 16384;
constexpr int L_Q = L_QP + 2048;
constexpr int L_O = L_Q + 2048;
constexpr int L_ML = L_O + 1024;
constexpr int L_SLIP = L_ML + 128;
constexpr int L_SC = L_SLIP + 16;
constexpr int L_BYTES = 84 * 1024;
static_assert(L_SC + 8 * 1024 <= L_BYTES && L_SC % 16 == 0, "lds map");

struct Params {
  const void *x0, *k0, *v0, *wq0, *wo0, *x1, *k1, *v1, *wq1, *wo1;
  void* out;
  unsigned char* ws;
};

__device__ __forceinline__ float dot2(unsigned a, unsigned b, float c) {
  return __builtin_amdgcn_fdot2_f32_bf16(__builtin_bit_cast(bf16x2, a), __builtin_bit_cast(bf16x2, b), c, false);
}
__device__ __forceinline__ unsigned pk_lo(unsigned a, unsigned b) { return __builtin_amdgcn_perm(b, a, 0x05040100u); }
__device__ __forceinline__ unsigned pk_hi(unsigned a, unsigned b) { return __builtin_amdgcn_perm(b, a, 0x07060302u); }
__device__ __forceinline__ unsigned cvtpk(float lo, float hi) {
  bf16x2 c = {(__bf16)lo, (__bf16)hi};
  return __builtin_bit_cast(unsigned, c);
}
template <int CTRL> __device__ __forceinline__ float dppf(float x) {
  return __builtin_bit_cast(float, __builtin_amdgcn_mov_dpp(__builtin_bit_cast(int, x), CTRL, 0xf, 0xf, true));
}
template <int CTRL> __device__ __forceinline__ unsigned dppu(unsigned x) {
  return (unsigned)__builtin_amdgcn_mov_dpp((int)x, CTRL, 0xf, 0xf, true);
}
constexpr int XOR1 = 0xB1, XOR2 = 0x4E, XOR7 = 0x141, ROR8 = 0x128, ROR4 = 0x124, ROR2 = 0x122, ROR1 = 0x121, BCAST = 0x150;
__device__ __forceinline__ float sum16(float a, float b) {
  auto r = __builtin_amdgcn_permlane16_swap(__float_as_uint(a), __float_as_uint(b), false, false);
  return __uint_as_float(r[0]) + __uint_as_float(r[1]);
}
__device__ __forceinline__ float sum32(float a, float b) {
  auto r = __builtin_amdgcn_permlane32_swap(__float_as_uint(a), __float_as_uint(b), false, false);
  return __uint_as_float(r[0]) + __uint_as_float(r[1]);
}
__device__ __forceinline__ float max32(float a) {
  auto r = __builtin_amdgcn_permlane32_swap(__float_as_uint(a), __float_as_uint(a), false, false);
  return fmaxf(__uint_as_float(r[0]), __uint_as_float(r[1]));
}
__device__ __forceinline__ unsigned enc_f(float f, unsigned tag) { return __builtin_amdgcn_alignbit(tag, __float_as_uint(f), 8); }
__device__ __forceinline__ float dec_f(unsigned d) { return __uint_as_float(d << 8); }
__device__ __forceinline__ unsigned diff4(v4u g, unsigned t) { return ((g.x ^ t) | (g.y ^ t)) | ((g.z ^ t) | (g.w ^ t)); }
__device__ __forceinline__ void wait_until(unsigned t) {
  SB();
  asm volatile("1:\n\ts_memrealtime vcc\n\ts_waitcnt lgkmcnt(0)\n\ts_sub_u32 vcc_lo, vcc_lo, %0\n\ts_cmp_lt_i32 vcc_lo, 0\n\t"
               "s_cbranch_scc0 2f\n\ts_sleep 1\n\ts_branch 1b\n2:" :: "s"(t) : "vcc", "scc", "memory");
  SB();
}

__device__ __forceinline__ void pace(unsigned t, int& lprev, int thr, unsigned slip_addr) {
  unsigned vt, slip, tgt;
  SB();
  asm volatile(
      "ds_read_b32 %0, %6\n\ts_memrealtime vcc\n\ts_waitcnt lgkmcnt(0)\n\t"
      "v_readfirstlane_b32 %1, %0\n\t"
      "s_add_u32 %2, %1, %4\n\t"
      "s_sub_u32 vcc_lo, vcc_lo, %2\n\t"
      "s_sub_i32 vcc_hi, vcc_lo, %3\n\t"
      "s_cmp_gt_i32 vcc_hi, %5\n\t"
      "s_cbranch_scc0 3f\n\t"
      "s_add_u32 %1, %1, %7\n\ts_add_u32 %2, %2, %7\n\ts_sub_u32 vcc_lo, vcc_lo, %7\n\t"
      "v_mov_b32 %0, %1\n\tds_max_u32 %6, %0\n"
      "3:\n\ts_max_i32 %3, vcc_lo, 0\n"
      "1:\n\ts_cmp_lt_i32 vcc_lo, 0\n\ts_cbranch_scc0 2f\n\ts_sleep 1\n\t"
      "s_memrealtime vcc\n\ts_waitcnt lgkmcnt(0)\n\ts_sub_u32 vcc_lo, vcc_lo, %2\n\ts_branch 1b\n"
      "2:\n\ts_waitcnt lgkmcnt(0)"
      : "=&v"(vt), "=&s"(slip), "=&s"(tgt), "+s"(lprev)
      : "s"(t), "s"(thr), "v"(slip_addr), "i"((SLIP_N * 4 * PACE_PER + 128) >> 8)
      : "vcc", "scc", "memory");
  SB();
}

#define STAMP(i) do { } while (0)
#define TRACE() do { } while (0)
#define LATE(x, tgt)
#define LATE2(x)

__global__ void __launch_bounds__(256, 1) decode_fused(Params p) {
  __shared__ __attribute__((aligned(16))) unsigned char smem[L_BYTES];
  LAS unsigned char* L = (LAS unsigned char*)smem;
  unsigned t0 = NOW();
  int probe = 0; const unsigned tt0 = t0;
  const int bid = blockIdx.x, kvh = bid >> 5, b = (bid >> 3) & 3, sp = bid & 7;
  const int br = bid >> 7, j = (bid >> 3) & 15, kr = (sp + KROT * br) & 7;
  const int qs = (j + QROT) & 15;
  const int cb = j & 3, rb = j >> 2;
  const int tid = threadIdx.x, lane = tid & 63, w = __builtin_amdgcn_readfirstlane(tid >> 6);
  const int voff = lane * 16, koff = tid * 16;
  const bool b3 = lane & 8, b2 = lane & 4, b1 = lane & 2, b0 = lane & 1;
  LAS float* const sc = (LAS float*)(L + L_SC + tid * 4);
  unsigned cpf;
  {
    unsigned long long pc;
    unsigned len;
    asm volatile("s_getpc_b64 %0\n.Ldec_code_begin:\n\ts_mov_b32 %1, .Ldec_code_end-.Ldec_code_begin" : "=s"(pc), "=s"(len));
    const auto rc = __builtin_amdgcn_make_buffer_rsrc((void*)pc, 0, len, RSRC_W3);
    cpf = __builtin_amdgcn_raw_buffer_load_b32(rc, tid < 8 ? tid * 128 : OOB, (bid >> 3) * 1024, 0);
  }
  asm volatile("" :: "s"(p.x0), "s"(p.k0), "s"(p.v0), "s"(p.wq0), "s"(p.wo0), "s"(p.x1), "s"(p.k1), "s"(p.v1), "s"(p.wq1), "s"(p.wo1), "s"(p.out), "s"(p.ws));
  const auto rws = __builtin_amdgcn_make_buffer_rsrc(p.ws, 0, WS_END, RSRC_W3);
  const auto ro = __builtin_amdgcn_make_buffer_rsrc(p.out, 0, 4 * 2 * DM * 2, RSRC_W3);

  const auto rwo = __builtin_amdgcn_make_buffer_rsrc((void*)(br ? p.wo1 : p.wo0), 0, NQ * DM * 2, RSRC_W3);
  const int wo_off = (kr * 512 + rb * 128 + 32 * w) * (DM * 2) + cb * 224;

  v4u R[16];
  float acc[2][8], m, l;
  unsigned tag;

#define QK_STEP(KR, i)                                                 \
  {                                                                    \
    float s0 = dot2(KR[i][0], q0[0], 0.f), s1 = dot2(KR[i][0], q1[0], 0.f); \
    s0 = dot2(KR[i][1], q0[1], s0); s1 = dot2(KR[i][1], q1[1], s1);    \
    s0 = dot2(KR[i][2], q0[2], s0); s1 = dot2(KR[i][2], q1[2], s1);    \
    s0 = dot2(KR[i][3], q0[3], s0); s1 = dot2(KR[i][3], q1[3], s1);    \
    y[i] = sum16(s0, s1);                                              \
  }
#define TR_STEP(H, CTRL, BIT)                                          \
  _Pragma("unroll") for (int i = 0; i < (H); ++i) {                    \
    const float keep = (BIT) ? y[i + (H)] : y[i], send = (BIT) ? y[i] : y[i + (H)]; \
    y[i] = keep + dppf<CTRL>(send);                                    \
  }
#define SCORES(slot)                                                   \
  TR_STEP(8, ROR8, b3) TR_STEP(4, XOR7, b2) TR_STEP(2, XOR2, b1) TR_STEP(1, XOR1, b0) \
  m = fmaxf(m, y[0]);                                                  \
  sc[(slot) * 256] = y[0];
#define PROBS(slot)                                                                     \
  const float pp = __builtin_amdgcn_exp2f((sc[(slot) * 256] - m) * SCL);                \
  l += pp;                                                                              \
  const unsigned ppk = cvtpk(pp, dppf<XOR1>(pp));        \
  const auto ph = __builtin_amdgcn_permlane16_swap(ppk, ppk, false, false);
#define PV_STEP(t)                                                     \
  {                                                                    \
    const unsigned p0 = dppu<BCAST + 2 * (t)>(ph[0]), p1 = dppu<BCAST + 2 * (t)>(ph[1]); \
    _Pragma("unroll") for (int k = 0; k < 4; ++k) {                    \
      const unsigned lo = pk_lo(R[2 * (t)][k], R[2 * (t) + 1][k]), hi = pk_hi(R[2 * (t)][k], R[2 * (t) + 1][k]); \
      acc[0][2 * k] = dot2(lo, p0, acc[0][2 * k]); acc[0][2 * k + 1] = dot2(hi, p0, acc[0][2 * k + 1]); \
      acc[1][2 * k] = dot2(lo, p1, acc[1][2 * k]); acc[1][2 * k + 1] = dot2(hi, p1, acc[1][2 * k + 1]); \
    }                                                                  \
  }
#define DUMP(brn)                                                      \
  {                                                                    \
    float o[8];                                                        \
    _Pragma("unroll") for (int k = 0; k < 8; ++k) o[k] = sum32(acc[0][k], acc[1][k]);     \
    LAS f32x4* mp = (LAS f32x4*)(L + L_MRG + (brn) * 8192 + tid * 32); \
    mp[0] = f32x4{o[0], o[1], o[2], o[3]};                             \
    mp[1] = f32x4{o[4], o[5], o[6], o[7]};                             \
    l += dppf<ROR8>(l); l += dppf<ROR4>(l); l += dppf<ROR2>(l); l += dppf<ROR1>(l); \
    l = sum32(l, l);                                                   \
    *(LAS f32x2*)(L + L_ML + (brn) * 64 + w * 16 + ((lane >> 4) & 1) * 8) = f32x2{m * SCL, l}; \
  }

  {
    const auto rx = __builtin_amdgcn_make_buffer_rsrc((void*)(br ? p.x1 : p.x0), 0, 4 * DM * 2, RSRC_W3);
    const auto rwq = __builtin_amdgcn_make_buffer_rsrc((void*)(br ? p.wq1 : p.wq0), 0, DM * NQ * 2, RSRC_W3);
    const size_t reg_off = (size_t)(b * 8 + kvh) * REGION_BYTES + sp * (REGION_BYTES / 8);

    v4u X[4], W[8], X0[16], X1[16];
    const unsigned ep = *(const __attribute__((address_space(4))) unsigned*)(p.ws + WS_EPOCH);
#define LDU(rs, i) __builtin_amdgcn_raw_buffer_load_b128(rs, koff, (i) * 4096, AUX_NT)
    const int poff = PACE_OFF * 256 + (((b * 8 + kvh) * PACE_MUL) & 31) * (PACE_STAG * PACE_PER / 32);
#define PACE(x)                                                                           \
  {                                                                                       \
    const unsigned tgt_ = t0 + ((min((int)(x), 512 - PACE_TAIL) * PACE_PER + poff) >> 8); \
    LATE(x, tgt_)                                                                         \
    wait_until(tgt_);                                                                     \
  }
    const auto rk0 = __builtin_amdgcn_make_buffer_rsrc((char*)p.k0 + reg_off, 0, 48 * 4096, RSRC_W3);
#define ISSUE(lo, hi)                                                                     \
  _Pragma("unroll") for (int i_ = (lo); i_ < (hi) && i_ < 48; ++i_) {                     \
    if (i_ >= H_A + 4 && (i_ & 3) == 0) PACE(i_)                                               \
    if (i_ < 16) X0[i_ & 15] = LDU(rk0, i_);                                              \
    else if (i_ < 32) X1[i_ & 15] = LDU(rk0, i_);                                         \
    else R[i_ & 15] = LDU(rk0, i_);                                                       \
    SB();                                                                                 \
  }
    {
      const int row0 = 128 * w + 8 * (lane >> 2);
      const int xoff = row0 < DM ? row0 * 2 : OOB;
#pragma unroll
      for (int i = 0; i < 4; ++i) X[i] = __builtin_amdgcn_raw_buffer_load_b128(rx, xoff, i * DM * 2, 0);
      const int woff = row0 * (NQ * 2) + (lane & 3) * 16;
#pragma unroll
      for (int n = 0; n < 8; ++n) {
        W[n] = __builtin_amdgcn_raw_buffer_load_b128(rwq, woff, (kr * 512 + qs * 32) * 2 + n * (NQ * 2), AUX_NT);
        SB();
        ISSUE(n * H_I / 8, (n + 1) * H_I / 8)
      }
    }
    ISSUE(H_I, H_A)

    {
      float a[4][8];
#pragma unroll
      for (int bb = 0; bb < 4; ++bb)
#pragma unroll
        for (int c = 0; c < 8; ++c) a[bb][c] = 0.f;
#pragma unroll
      for (int t = 0; t < 4; ++t) {
#pragma unroll
        for (int k = 0; k < 4; ++k) {
          const unsigned lo = pk_lo(W[2 * t][k], W[2 * t + 1][k]), hi = pk_hi(W[2 * t][k], W[2 * t + 1][k]);
#pragma unroll
          for (int bb = 0; bb < 4; ++bb) {
            a[bb][2 * k] = dot2(lo, X[bb][t], a[bb][2 * k]);
            a[bb][2 * k + 1] = dot2(hi, X[bb][t], a[bb][2 * k + 1]);
          }
        }
        SB();
        ISSUE(H_A + t, H_A + t + 1)
      }
      asm volatile("" :: "v"(cpf));
      STAMP(14);
      float y[8];
#pragma unroll
      for (int c = 0; c < 8; ++c) {
        y[c] = sum16(sum32(a[0][c], a[2][c]), sum32(a[1][c], a[3][c]));
        y[c] += dppf<ROR8>(y[c]);
        y[c] += dppf<ROR4>(y[c]);
      }
      LAS f32x4* qp = (LAS f32x4*)(L + L_QP + w * 512 + (lane >> 4) * 128 + (lane & 3) * 32);
      qp[0] = f32x4{y[0], y[1], y[2], y[3]};
      qp[1] = f32x4{y[4], y[5], y[6], y[7]};
    }
    STAMP(1);
    __syncthreads();
    tag = __builtin_amdgcn_readfirstlane(ep) + 1u;
    if (w == 0) {
      const unsigned tt = ((NOW() >> 2) & 0xffu) | tag << 8;
      const LAS f32x4* qp = (const LAS f32x4*)(L + L_QP + (lane & 31) * 16);
      const f32x4 q = (qp[0] + qp[32]) + (qp[64] + qp[96]);
      const unsigned lo = cvtpk(q.x, q.y), hi = cvtpk(q.z, q.w);
      __builtin_amdgcn_raw_buffer_store_b128(v4u{pk_lo(lo, tt), __builtin_amdgcn_alignbit(tt, lo, 16), pk_lo(hi, tt), __builtin_amdgcn_alignbit(tt, hi, 16)},
                                             rws, lane < 32 ? voff : OOB, WS_QG + (kr * 32 + br * 16 + qs) * 512, AUX_SC1);
    }
    SB();
    STAMP(2);
    ISSUE(H_A + 4, H_P)
    const int qg_off = WS_QG + kvh * 32 * 512 + b * 128, qg_v = (tid >> 3) * 512 + (tid & 7) * 16;
    const int ck_v = (lane & 15) * (16 * 512) + 2 * 512;
    v4u qg = __builtin_amdgcn_raw_buffer_load_b128(rws, qg_v, qg_off, AUX_SC1);
    unsigned ck = __builtin_amdgcn_raw_buffer_load_b32(rws, ck_v, WS_QG, AUX_SC1);
    SB();
    STAMP(3);
    ISSUE(H_P, H_C)
    STAMP(15);
    if (__any((diff4(qg, tag << 24) | (ck ^ (tag << 24))) >> 24)) {
#pragma unroll 1
      for (unsigned spins = 0; spins < SPIN_CAP; ++spins) {
        MEMFENCE();
        qg = __builtin_amdgcn_raw_buffer_load_b128(rws, qg_v, qg_off, AUX_SC1);
        ck = __builtin_amdgcn_raw_buffer_load_b32(rws, ck_v, WS_QG, AUX_SC1);
        if (!__any((diff4(qg, tag << 24) | (ck ^ (tag << 24))) >> 24)) break;
        __builtin_amdgcn_s_sleep(2);
      }
    }
    *(LAS v2u*)(L + L_Q + tid * 8) = v2u{pk_lo(qg.x, qg.y), pk_lo(qg.z, qg.w)};
    *(LAS unsigned*)(L + L_SLIP) = 0u;
    {
      const unsigned ref = (t0 + PUB_NOM) & ~3u;
      float d = (float)(int)(signed char)((ck >> 16) - (ref >> 2));
      d += dppf<ROR8>(d); d += dppf<ROR4>(d); d += dppf<ROR2>(d); d += dppf<ROR1>(d);
      const unsigned ta = ref + (unsigned)(__builtin_amdgcn_readfirstlane((int)d) >> 2) - PUB_NOM;
      t0 += min(max((int)(ta - t0), -150), 400);
    }
    STAMP(4);
    __syncthreads();
    v4u q0 = *(const LAS v4u*)(L + L_Q + (lane & 31) * 16), q1 = *(const LAS v4u*)(L + L_Q + 512 + (lane & 31) * 16);

    m = -INFINITY;
    l = 0.f;
#pragma unroll
    for (int k = 0; k < 8; ++k) acc[0][k] = acc[1][k] = 0.f;
    {
      float y[16];
#pragma unroll
      for (int i = 0; i < 16; ++i) {
        if (i % H_S == 0) { SB(); ISSUE(H_C + (i / H_S) * 4, H_C + (i / H_S) * 4 + 4) }
        QK_STEP(X0, i)
      }
      SCORES(0)
      STAMP(17);
#pragma unroll
      for (int i = 0; i < 16; ++i) {
        if ((i + 16) % H_S == 0) { SB(); ISSUE(H_C + ((i + 16) / H_S) * 4, H_C + ((i + 16) / H_S) * 4 + 4) }
        QK_STEP(X1, i)
      }
      SCORES(1)
      SB();
      STAMP(18);
    }
    ISSUE(H_C + (31 / H_S + 1) * 4, 48)
    STAMP(5);
    TRACE(); TRACE();
    int budget = EARLY;
    const unsigned slip_addr = (unsigned)(unsigned long)(L + L_SLIP);
    int lprev = 1 << 20;
#define BURST(g)                                                       \
  { const int inc_ = min(budget, EARLY_STEP); t0 += inc_; budget -= inc_; } \
  pace(t0 + ((min(x + 4 * (g), 512 - PACE_TAIL) * PACE_PER + poff) >> 8), lprev, x < 112 ? SLIP_THR0 : SLIP_THR, slip_addr); \
  LATE2(x + 4 * (g))                                                    \
  _Pragma("unroll") for (int u_ = 4 * (g); u_ < 4 * (g) + 4; ++u_) { R[u_] = LDU(rn, u_); SB(); }
    const char *kp = (const char*)p.k0 + reg_off, *vp = (const char*)p.v0 + reg_off;
    int x = 48, g = 2;
#pragma unroll 1
    for (int grp = 0;; ++grp) {
#pragma unroll 1
      do {
        const auto rn = __builtin_amdgcn_make_buffer_rsrc((void*)(g < 7 ? kp + (g + 1) * 65536 : vp), 0, 65536, RSRC_W3);
        float y[16];
#pragma unroll
        for (int i = 0; i < 16; ++i) {
          QK_STEP(R, i)
          if ((i & 3) == 3) { BURST(i >> 2) }
        }
        SCORES(g)
        SB();
        TRACE();
        probe = x == 160 ? lprev : probe;
        x += 16;
      } while (++g < 8);
      m = fmaxf(m, dppf<ROR8>(m)); m = fmaxf(m, dppf<ROR4>(m)); m = fmaxf(m, dppf<ROR2>(m)); m = fmaxf(m, dppf<ROR1>(m));
      m = max32(m);
      kp = (const char*)p.k1 + reg_off;
      budget += GAP_T;
      g = 0;
#pragma unroll 1
      do {
        const auto rn = __builtin_amdgcn_make_buffer_rsrc((void*)(g < 7 ? vp + (g + 1) * 65536 : kp), 0, 65536, RSRC_W3);
        PROBS(g)
        PV_STEP(0) PV_STEP(1) BURST(0) PV_STEP(2) PV_STEP(3) BURST(1) PV_STEP(4) PV_STEP(5) BURST(2) PV_STEP(6) PV_STEP(7) BURST(3)
        TRACE();
        probe = x == 160 ? lprev : probe;
        x += 16;
      } while (++g < 8 - grp);
      if (grp) break;
      DUMP(0)
#pragma unroll
      for (int k = 0; k < 8; ++k) acc[0][k] = acc[1][k] = 0.f;
      m = -INFINITY;
      l = 0.f;
      q0 = *(const LAS v4u*)(L + L_Q + 1024 + (lane & 31) * 16);
      q1 = *(const LAS v4u*)(L + L_Q + 1536 + (lane & 31) * 16);
      vp = (const char*)p.v1 + reg_off;
      budget += GAP_T;
      g = 0;
    }
    STAMP(19);
  }
  v4u WO[8];
  {
    const int ooff = (lane & 15) < 14 ? (lane >> 4) * (DM * 2) + (lane & 15) * 16 : OOB;
#define LAST_STEP(t)                                                                      \
  if ((t) >= WO_AT) {                                                                     \
    SB();                                                                                 \
    _Pragma("unroll") for (int n = ((t) - WO_AT) * WO_N; n < ((t) == 8 ? 8 : ((t) - WO_AT + 1) * WO_N) && n < 8; ++n) \
      WO[n] = __builtin_amdgcn_raw_buffer_load_b128(rwo, ooff, wo_off + 4 * n * (DM * 2), AUX_NT); \
    SB();                                                                                 \
  }                                                                                       \
  if ((t) < 8) PV_STEP((t) & 7)
    PROBS(7)
    LAST_STEP(0) LAST_STEP(1) LAST_STEP(2) LAST_STEP(3) LAST_STEP(4) LAST_STEP(5) LAST_STEP(6) LAST_STEP(7) LAST_STEP(8)
    TRACE();
  }
  STAMP(6);
  const unsigned tag24 = tag << 24;

  DUMP(1)
  LDS_BARRIER();
  {
    f32x2 ml[4];
    f32x4 ov[4];
#pragma unroll
    for (int i = 0; i < 4; ++i) {
      ml[i] = *(const LAS f32x2*)(L + L_ML + (w >> 1) * 64 + i * 16 + (w & 1) * 8);
      ov[i] = *(const LAS f32x4*)(L + L_MRG + (w >> 1) * 8192 + i * 2048 + (w & 1) * 1024 + voff);
    }
    const float M = __uint_as_float(__float_as_uint(fmaxf(fmaxf(ml[0].x, ml[1].x), fmaxf(ml[2].x, ml[3].x))) & 0xffffff00u);
    f32x4 o = {0.f, 0.f, 0.f, 0.f};
    float ls = 0.f;
#pragma unroll
    for (int i = 0; i < 4; ++i) {
      const float e = __builtin_amdgcn_exp2f(ml[i].x - M);
      o += e * ov[i];
      ls = fmaf(e, ml[i].y, ls);
    }
    __builtin_amdgcn_raw_buffer_store_b128(v4u{enc_f(o.x, tag), enc_f(o.y, tag), enc_f(o.z, tag), enc_f(o.w, tag)}, rws, voff,
                                           WS_OG + ((w >> 1) * 256 + bid) * 2048 + (w & 1) * 1024, AUX_SC1);
    __builtin_amdgcn_raw_buffer_store_b128(v4u{enc_f(M, tag), tag, enc_f(ls, tag), tag}, rws, lane == 0 ? 0 : OOB,
                                           WS_ML + ((w >> 1) * 32 + (bid >> 3)) * 256 + (w & 1) * 128 + sp * 16, AUX_SC1);
  }
  STAMP(7);
  DRAIN();
  STAMP(12);

  v4u BW[2][4];
  {
    v4u g[4], gm;
    const int og_off = WS_OG + (br * 256 + kr * 32 + w * 8) * 2048 + rb * 512, og_v = (lane >> 5) * 2048 + (lane & 31) * 16;
    const int ml_off = WS_ML + (br * 32 + kr * 4 + w) * 256 + (rb >> 1) * 128, ml_v = (((lane & 3) * 2 + (lane >> 5)) ^ ((lane >> 2) & 1)) * 16;
#define ML_BAD() ((((gm.x ^ tag24) | (gm.z ^ tag24)) >> 24) | (gm.y ^ tag) | (gm.w ^ tag))
#define O_BAD() ((diff4(g[0], tag24) | diff4(g[1], tag24) | diff4(g[2], tag24) | diff4(g[3], tag24)) >> 24)
#define GATHER()                                                                  \
  gm = __builtin_amdgcn_raw_buffer_load_b128(rws, ml_v, ml_off, AUX_SC1);         \
  _Pragma("unroll") for (int s = 0; s < 4; ++s) g[s] = __builtin_amdgcn_raw_buffer_load_b128(rws, og_v, og_off + s * 4096, AUX_SC1);
    GATHER()
    SB();
#pragma unroll
    for (int n = 0; n < 8; ++n) *(LAS v4u*)(L + L_WO + (32 * w + 4 * n) * 256 + voff) = WO[n];
    LDS_BARRIER();
#pragma unroll
    for (int c = 0; c < 2; ++c)
#pragma unroll
      for (int ks = 0; ks < 4; ++ks) {
        LAS unsigned char* a = L + L_WO + (32 * ks + 8 * (lane >> 4) + ((lane >> 2) & 3)) * 256 + (2 * w + c) * 32 + (lane & 3) * 8;
        const v2u r0 = __builtin_bit_cast(v2u, __builtin_amdgcn_ds_read_tr16_b64_v4i16((LAS v4s*)a));
        const v2u r1 = __builtin_bit_cast(v2u, __builtin_amdgcn_ds_read_tr16_b64_v4i16((LAS v4s*)(a + 1024)));
        BW[c][ks] = v4u{r0.x, r0.y, r1.x, r1.y};
      }
    SB();
    if (__any(ML_BAD() | O_BAD())) {
#pragma unroll 1
      for (unsigned spins = 0; spins < SPIN_CAP; ++spins) {
        MEMFENCE();
        GATHER()
        if (!__any(ML_BAD() | O_BAD())) break;
        __builtin_amdgcn_s_sleep(SLEEP_A);
      }
    }
    const float Ms = dec_f(gm.x), Ls = dec_f(gm.z);
    float Mx = fmaxf(Ms, dppf<XOR1>(Ms));
    Mx = fmaxf(Mx, dppf<XOR2>(Mx));
    Mx = fmaxf(Mx, dppf<XOR7>(Mx));
    const float e = __builtin_amdgcn_exp2f(Ms - Mx);
    float Lt = e * Ls;
    Lt += dppf<XOR1>(Lt);
    Lt += dppf<XOR2>(Lt);
    Lt += dppf<XOR7>(Lt);
    const float c = e * __builtin_amdgcn_rcpf(Lt);
    float o0 = 0.f, o1 = 0.f, o2 = 0.f, o3 = 0.f;
#define MRG(s) { const float cs = dppf<BCAST + s>(c); o0 = fmaf(cs, dec_f(g[s].x), o0); o1 = fmaf(cs, dec_f(g[s].y), o1); o2 = fmaf(cs, dec_f(g[s].z), o2); o3 = fmaf(cs, dec_f(g[s].w), o3); }
    MRG(0) MRG(1) MRG(2) MRG(3)
    *(LAS unsigned*)(L + L_O + w * 256 + (lane & 31) * 8 + (lane >> 5) * 4) = cvtpk(sum32(o0, o2), sum32(o1, o3));
  }
  STAMP(8);
  LDS_BARRIER();
  {
    f32x4 d0 = {0.f, 0.f, 0.f, 0.f}, d1 = {0.f, 0.f, 0.f, 0.f};
#pragma unroll
    for (int ks = 0; ks < 4; ++ks) {
      const v4u o = *(const LAS v4u*)(L + L_O + (lane & 3) * 256 + ks * 64 + (lane >> 4) * 16);
      d0 = __builtin_amdgcn_mfma_f32_16x16x32_bf16(__builtin_bit_cast(bf16x8, BW[0][ks]), __builtin_bit_cast(bf16x8, o), d0, 0, 0, 0);
      d1 = __builtin_amdgcn_mfma_f32_16x16x32_bf16(__builtin_bit_cast(bf16x8, BW[1][ks]), __builtin_bit_cast(bf16x8, o), d1, 0, 0, 0);
    }
    const f32x4 d = b2 ? d1 : d0;
    __builtin_amdgcn_raw_buffer_store_b128(v4u{enc_f(d.x, tag), enc_f(d.y, tag), enc_f(d.z, tag), enc_f(d.w, tag)}, rws,
                                           b3 ? OOB : (lane & 3) * 4096 + (lane & 4) * 16 + (lane >> 4) * 16,
                                           WS_PG + ((br * 4 + cb) * 4 + w) * 16384 + (kr * 4 + rb) * 128, AUX_SC1);
  }
  STAMP(9);

  if (w == 3 && (bid & 1) == 0) {
    const int ri = bid >> 1;
    v4u g[4];
    DRAIN();
#pragma unroll 1
    for (unsigned spins = 0; spins < SPIN_CAP; ++spins) {
      MEMFENCE();
#pragma unroll
      for (int s = 0; s < 4; ++s) g[s] = __builtin_amdgcn_raw_buffer_load_b128(rws, voff + s * 1024, WS_PG + ri * 4096, AUX_SC1);
      if (!__any((diff4(g[0], tag24) | diff4(g[1], tag24) | diff4(g[2], tag24) | diff4(g[3], tag24)) >> 24)) break;
      __builtin_amdgcn_s_sleep(SLEEP_B);
    }
    float sum[4];
#pragma unroll
    for (int k = 0; k < 4; ++k) {
      sum[k] = (dec_f(g[0][k]) + dec_f(g[1][k])) + (dec_f(g[2][k]) + dec_f(g[3][k]));
      sum[k] += dppf<ROR8>(sum[k]);
      sum[k] = sum16(sum[k], sum[k]);
      sum[k] = sum32(sum[k], sum[k]);
    }
    __builtin_amdgcn_raw_buffer_store_b64(v2u{cvtpk(sum[0], sum[1]), cvtpk(sum[2], sum[3])}, ro, lane < ((ri & 12) == 12 ? 4 : 8) ? lane * 8 : OOB,
                                          ((ri & 3) * 2 * DM + (ri >> 6) * DM + ((ri >> 4) & 3) * 112 + ((ri >> 2) & 3) * 32) * 2, 0);
    __builtin_amdgcn_raw_buffer_store_b32(tag, rws, (bid == 0 && lane == 0) ? 0 : OOB, WS_EPOCH, AUX_SC1);
  }
  while ((true) && (int)(NOW() - tt0) < 10000 + (5 * probe)) __builtin_amdgcn_s_sleep(1);
  asm volatile(".Ldec_code_end:");
}

}

extern "C" void kernel_launch(void* const* d_in, const int* in_sizes, int n_in, void* d_out, int out_size, void* d_ws, size_t ws_size,
                              hipStream_t stream) {
  Params p;
  p.x0 = d_in[0]; p.k0 = d_in[1]; p.v0 = d_in[2]; p.wq0 = d_in[3]; p.wo0 = d_in[4];
  p.x1 = d_in[5]; p.k1 = d_in[6]; p.v1 = d_in[7]; p.wq1 = d_in[8]; p.wo1 = d_in[9];
  p.out = d_out;
  p.ws = (unsigned char*)d_ws;
  decode_fused<<<256, 256, 0, stream>>>(p);
}
```

```cpp
#include <hip/hip_runtime.h>
#include <hip/hip_bf16.h>
#include <stdint.h>

namespace {

typedef unsigned v4u __attribute__((ext_vector_type(4)));
typedef unsigned v2u __attribute__((ext_vector_type(2)));
typedef unsigned long long v8p __attribute__((ext_vector_type(8)));
typedef unsigned long long v4p __attribute__((ext_vector_type(4)));
typedef float f32x4 __attribute__((ext_vector_type(4)));
typedef float f32x2 __attribute__((ext_vector_type(2)));
typedef short v4s __attribute__((ext_vector_type(4)));
typedef __bf16 bf16x2 __attribute__((ext_vector_type(2)));
typedef __bf16 bf16x8 __attribute__((ext_vector_type(8)));
#define LAS __attribute__((address_space(3)))
#define SB() __builtin_amdgcn_sched_barrier(0)
#define MEMFENCE() asm volatile("" ::: "memory")
#define KEEP(x) asm volatile("" :: "v"(x))
#define DRAIN() asm volatile("s_waitcnt vmcnt(0)" ::: "memory")
#define LDS_BARRIER() asm volatile("s_waitcnt lgkmcnt(0)\n\ts_barrier" ::: "memory")
#define NOW() ((unsigned)__builtin_amdgcn_s_memrealtime())

constexpr int DM = 448, NQ = 4096, SK = 8192, DH = 256;
constexpr unsigned REGION_BYTES = SK * DH * 2;

constexpr int PACE_GBS = 7800;
constexpr int PACE_OFF = 145;
constexpr int PACE_REL = 510;
constexpr int PUB_NOM = 207;
constexpr int EARLY = 55;
constexpr int EARLY_STEP = 4;
constexpr int SK_0 = 45;
constexpr int SK_K = 2;
constexpr int GAP_T = 0;
constexpr int SLIP_THR = 90;
constexpr int SLIP_N = 5;
constexpr int H_A = 8;
constexpr int H_P = 24;
constexpr int H_C = 32;
constexpr int H_S = 11;
constexpr int WO_A1 = 7;
constexpr int WO_A2 = 9;
constexpr int WO_E = 2;
constexpr int WO_T = (WO_E * 13);
constexpr int T_GATHER = 150;
constexpr int T_REDUCE = 340;
constexpr int PACE_PER = (int)(1048576.0 * 256.0 / (PACE_GBS * 10.0) + 0.5);
static_assert(H_C >= 32 && H_C <= 44 && H_P <= H_C && H_A % 4 == 0 && H_P % 4 == 0 && H_C % 4 == 0, "head schedule");

constexpr int AUX_NT = 2, AUX_SC1 = 16, RSRC_W3 = 0x00020000;
constexpr int OOB = 0x7ffffff0;
constexpr unsigned SPIN_CAP = 1u << 18;
constexpr float SCL = 0.0625f * 1.4426950408889634f;

constexpr unsigned WS_EPOCH = 0;
constexpr unsigned WS_QG = 32768;
constexpr unsigned OG_STR = 2048 + 128;
constexpr unsigned WS_OG = WS_QG + 256 * 512;
constexpr unsigned WS_PG = WS_OG + 2 * 256 * OG_STR;
constexpr unsigned WS_END = WS_PG + 128 * 4096;

constexpr int WO_STR = 288;
constexpr int L_WO = 0;
constexpr int L_MRG = 128 * WO_STR;
constexpr int L_QP = L_MRG + 16384;
constexpr int L_Q = L_QP + 2048;
constexpr int L_O = L_Q + 2048;
constexpr int L_ML = L_O + 1024;
constexpr int L_SLIP = L_ML + 128;
constexpr int L_SC = L_SLIP + 16;
constexpr int L_BYTES = 84 * 1024;
static_assert(L_SC + 8 * 1024 <= L_BYTES && L_SC % 16 == 0, "lds map");

struct Params {
  const void *x0, *k0, *v0, *wq0, *wo0, *x1, *k1, *v1, *wq1, *wo1;
  void* out;
  unsigned char* ws;
};

__device__ __forceinline__ float dot2(unsigned a, unsigned b, float c) {
  return __builtin_amdgcn_fdot2_f32_bf16(__builtin_bit_cast(bf16x2, a), __builtin_bit_cast(bf16x2, b), c, false);
}
__device__ __forceinline__ unsigned pk_lo(unsigned a, unsigned b) { return __builtin_amdgcn_perm(b, a, 0x05040100u); }
__device__ __forceinline__ unsigned pk_hi(unsigned a, unsigned b) { return __builtin_amdgcn_perm(b, a, 0x07060302u); }
__device__ __forceinline__ unsigned cvtpk(float lo, float hi) {
  bf16x2 c = {(__bf16)lo, (__bf16)hi};
  return __builtin_bit_cast(unsigned, c);
}
template <int CTRL> __device__ __forceinline__ float dppf(float x) {
  return __builtin_bit_cast(float, __builtin_amdgcn_mov_dpp(__builtin_bit_cast(int, x), CTRL, 0xf, 0xf, true));
}
template <int CTRL> __device__ __forceinline__ unsigned dppu(unsigned x) {
  return (unsigned)__builtin_amdgcn_mov_dpp((int)x, CTRL, 0xf, 0xf, true);
}
constexpr int XOR1 = 0xB1, XOR2 = 0x4E, XOR7 = 0x141, ROR8 = 0x128, ROR4 = 0x124, ROR2 = 0x122, ROR1 = 0x121, BCAST = 0x150;
__device__ __forceinline__ float row_sum(float a) {
  a += dppf<ROR8>(a); a += dppf<ROR4>(a); a += dppf<ROR2>(a);
  return a + dppf<ROR1>(a);
}
__device__ __forceinline__ float row_max(float a) {
  a = fmaxf(a, dppf<ROR8>(a)); a = fmaxf(a, dppf<ROR4>(a)); a = fmaxf(a, dppf<ROR2>(a));
  return fmaxf(a, dppf<ROR1>(a));
}
__device__ __forceinline__ float sum16(float a, float b) {
  auto r = __builtin_amdgcn_permlane16_swap(__float_as_uint(a), __float_as_uint(b), false, false);
  return __uint_as_float(r[0]) + __uint_as_float(r[1]);
}
__device__ __forceinline__ float sum32(float a, float b) {
  auto r = __builtin_amdgcn_permlane32_swap(__float_as_uint(a), __float_as_uint(b), false, false);
  return __uint_as_float(r[0]) + __uint_as_float(r[1]);
}
__device__ __forceinline__ float max32(float a) {
  auto r = __builtin_amdgcn_permlane32_swap(__float_as_uint(a), __float_as_uint(a), false, false);
  return fmaxf(__uint_as_float(r[0]), __uint_as_float(r[1]));
}
constexpr unsigned TAG_INC = 0x35;
__device__ __forceinline__ unsigned enc_f(float f, unsigned tag) { return __builtin_amdgcn_alignbit(tag, __float_as_uint(f), 8); }
__device__ __forceinline__ v4u enc4(f32x4 f, unsigned tag) {
  return v4u{enc_f(f.x, tag), enc_f(f.y, tag + TAG_INC), enc_f(f.z, tag + 2 * TAG_INC), enc_f(f.w, tag + 3 * TAG_INC)};
}
__device__ __forceinline__ float dec_f(unsigned d) { return __uint_as_float(d << 8); }
__device__ __forceinline__ unsigned diff4(v4u g, unsigned tag) {
  return ((g.x ^ (tag << 24)) | (g.y ^ ((tag + TAG_INC) << 24))) | ((g.z ^ ((tag + 2 * TAG_INC) << 24)) | (g.w ^ ((tag + 3 * TAG_INC) << 24)));
}

__device__ __forceinline__ void wait_until(unsigned t) {
  SB();
  asm volatile("1:\n\ts_memrealtime vcc\n\ts_waitcnt lgkmcnt(0)\n\ts_sub_u32 vcc_lo, vcc_lo, %0\n\ts_cmp_lt_i32 vcc_lo, 0\n\t"
               "s_cbranch_scc0 2f\n\ts_sleep 1\n\ts_branch 1b\n2:" :: "s"(__builtin_amdgcn_readfirstlane(t)) : "vcc", "scc", "memory");
  SB();
}
__device__ __forceinline__ void pace(unsigned t, int& lprev, unsigned slip_addr) {
  unsigned vt, slip, tgt;
  int lp = __builtin_amdgcn_readfirstlane(lprev);
  SB();
  asm volatile(
      "ds_read_b32 %0, %5\n\ts_memrealtime vcc\n\ts_waitcnt lgkmcnt(0)\n\t"
      "v_readfirstlane_b32 %1, %0\n\t"
      "s_add_u32 %2, %1, %4\n\t"
      "s_sub_u32 vcc_lo, vcc_lo, %2\n\t"
      "s_lshl_b32 %3, %3, 4\n\ts_sub_i32 vcc_hi, vcc_lo, %3\n\t"
      "s_cmp_gt_i32 vcc_hi, %7\n\t"
      "s_cbranch_scc0 3f\n\t"
      "s_add_u32 %1, %1, %6\n\ts_add_u32 %2, %2, %6\n\ts_sub_u32 vcc_lo, vcc_lo, %6\n\t"
      "v_mov_b32 %0, %1\n\tds_max_u32 %5, %0\n"
      "3:\n\ts_max_i32 %3, vcc_lo, 0\n"
      "1:\n\ts_cmp_lt_i32 vcc_lo, 0\n\ts_cbranch_scc0 2f\n\ts_sleep 1\n\t"
      "s_memrealtime vcc\n\ts_waitcnt lgkmcnt(0)\n\ts_sub_u32 vcc_lo, vcc_lo, %2\n\ts_branch 1b\n"
      "2:\n\ts_waitcnt lgkmcnt(0)"
      : "=&v"(vt), "=&s"(slip), "=&s"(tgt), "+s"(lp)
      : "s"(__builtin_amdgcn_readfirstlane(t)), "v"(slip_addr), "i"((SLIP_N * 4 * PACE_PER + 128) >> 8), "i"(SLIP_THR)
      : "vcc", "scc", "memory");
  lprev = __builtin_amdgcn_readfirstlane(lp);
  SB();
}


__global__ void __launch_bounds__(256, 1) decode_fused(Params args) {
  __shared__ __attribute__((aligned(16))) unsigned char smem[L_BYTES];
  LAS unsigned char* L = (LAS unsigned char*)smem;
  v8p arg_a;
  v4p arg_b;
  unsigned long long clk0;
  asm volatile("s_load_dwordx16 %0, %3, 0x0\n\ts_load_dwordx8 %1, %3, 0x40\n\ts_memrealtime %2"
               : "=&s"(arg_a), "=&s"(arg_b), "=&s"(clk0) : "s"(__builtin_amdgcn_kernarg_segment_ptr()));
  SB();
  const int bid = blockIdx.x, kvh = bid >> 5, b = (bid >> 3) & 3, sp = bid & 7;
  const int br = bid >> 7, j = (bid >> 3) & 15, kr = (sp + 4 * br) & 7, cb = j & 3, rb = j >> 2;
  const int tid = threadIdx.x, lane = tid & 63, w = __builtin_amdgcn_readfirstlane(tid >> 6);
  const int voff = lane * 16, koff = tid * 16;
  const bool b3 = lane & 8, b2 = lane & 4, b1 = lane & 2, b0 = lane & 1;
  LAS float* const sc = (LAS float*)(L + L_SC + tid * 4);

  unsigned cpf;
  {
    unsigned long long pc;
    unsigned len;
    asm volatile("s_getpc_b64 %0\n.Ldec_code_begin:\n\ts_mov_b32 %1, .Ldec_code_end-.Ldec_code_begin" : "=s"(pc), "=s"(len));
    const auto rc = __builtin_amdgcn_make_buffer_rsrc((void*)pc, 0, len, RSRC_W3);
    cpf = __builtin_amdgcn_raw_buffer_load_b32(rc, tid < 8 ? tid * 128 : OOB, (bid >> 3) * 1024, 0);
  }
  const int row0 = 128 * w + 8 * (lane >> 2);
  int xoff = row0 < DM ? row0 * 2 : OOB, woff = row0 * (NQ * 2) + (lane & 3) * 16, wso = (kr * 512 + j * 32) * 2;
  asm volatile("" : "+v"(xoff), "+v"(woff));
  SB();
  asm volatile("s_waitcnt lgkmcnt(0)" : "+s"(arg_a), "+s"(arg_b), "+s"(clk0), "+s"(wso));
  SB();
  Params p;
  p.x0 = (void*)arg_a[0]; p.k0 = (void*)arg_a[1]; p.v0 = (void*)arg_a[2]; p.wq0 = (void*)arg_a[3]; p.wo0 = (void*)arg_a[4];
  p.x1 = (void*)arg_a[5]; p.k1 = (void*)arg_a[6]; p.v1 = (void*)arg_a[7]; p.wq1 = (void*)arg_b[0]; p.wo1 = (void*)arg_b[1];
  p.out = (void*)arg_b[2]; p.ws = (unsigned char*)arg_b[3];
  unsigned t0 = (unsigned)clk0;
  const auto rws = __builtin_amdgcn_make_buffer_rsrc(p.ws, 0, WS_END, RSRC_W3);
  const size_t reg_off = (size_t)(b * 8 + kvh) * REGION_BYTES + sp * (REGION_BYTES / 8);
  const char* const wop = (const char*)(br ? p.wo1 : p.wo0) + (kr * 512 + rb * 128) * (DM * 2) + cb * 224;

  v4u X[4], W[8], X0[16], X1[16], R[16];
  unsigned ep;
  {
    const auto rx = __builtin_amdgcn_make_buffer_rsrc((void*)(br ? p.x1 : p.x0), 0, 4 * DM * 2, RSRC_W3);
    const auto rwq = __builtin_amdgcn_make_buffer_rsrc((void*)(br ? p.wq1 : p.wq0), 0, DM * NQ * 2, RSRC_W3);
#pragma unroll
    for (int i = 0; i < 4; ++i) X[i] = __builtin_amdgcn_raw_buffer_load_b128(rx, xoff, i * DM * 2, 0);
    ep = __builtin_amdgcn_raw_buffer_load_b32(rws, 0, w == 0 ? WS_EPOCH : OOB, AUX_SC1);
    SB();
#pragma unroll
    for (int n = 0; n < 8; ++n) W[n] = __builtin_amdgcn_raw_buffer_load_b128(rwq, woff, wso + n * (NQ * 2), AUX_NT);
    SB();
  }
  unsigned tch;
  {
    const size_t a12 = w == 1 ? (size_t)p.v0 : (size_t)p.k1, a03 = w == 3 ? (size_t)p.v1 : (size_t)wop;
    const size_t a = (w == 1 || w == 2 ? a12 : a03) + (w ? reg_off : 0);
    tch = __builtin_amdgcn_raw_buffer_load_b32(__builtin_amdgcn_make_buffer_rsrc((void*)a, 0, 65536, RSRC_W3), lane == 0 ? ((bid * 3 + w) & 63) * 896 : OOB, 0, AUX_NT);
    SB();
  }
#define LDU(rs, i) __builtin_amdgcn_raw_buffer_load_b128(rs, koff, (i) * 4096, AUX_NT)
  const int poff = PACE_OFF * 256 + (((b * 8 + kvh) * 19) & 31) * (4 * PACE_PER / 32);
  const auto rk0 = __builtin_amdgcn_make_buffer_rsrc((char*)p.k0 + reg_off, 0, 48 * 4096, RSRC_W3);
#define ISSUE(lo, hi)                                                                     \
  _Pragma("unroll") for (int i_ = (lo); i_ < (hi) && i_ < 48; ++i_) {                     \
    if (i_ >= H_A + 4 && (i_ & 3) == 0) wait_until(t0 + ((i_ * PACE_PER + poff) >> 8));   \
    if (i_ < 16) X0[i_ & 15] = LDU(rk0, i_);                                              \
    else if (i_ < 32) X1[i_ & 15] = LDU(rk0, i_);                                         \
    else R[i_ & 15] = LDU(rk0, i_);                                                       \
    SB();                                                                                 \
  }
  ISSUE(0, H_A)

  {
    float a[4][8];
#pragma unroll
    for (int bb = 0; bb < 4; ++bb)
#pragma unroll
      for (int c = 0; c < 8; ++c) a[bb][c] = 0.f;
#pragma unroll
    for (int t = 0; t < 4; ++t) {
#pragma unroll
      for (int k = 0; k < 4; ++k) {
        const unsigned lo = pk_lo(W[2 * t][k], W[2 * t + 1][k]), hi = pk_hi(W[2 * t][k], W[2 * t + 1][k]);
#pragma unroll
        for (int bb = 0; bb < 4; ++bb) {
          a[bb][2 * k] = dot2(lo, X[bb][t], a[bb][2 * k]);
          a[bb][2 * k + 1] = dot2(hi, X[bb][t], a[bb][2 * k + 1]);
        }
      }
      SB();
      ISSUE(H_A + t, H_A + t + 1)
    }
    float y[8];
#pragma unroll
    for (int c = 0; c < 8; ++c) {
      y[c] = sum16(sum32(a[0][c], a[2][c]), sum32(a[1][c], a[3][c]));
      y[c] += dppf<ROR8>(y[c]);
      y[c] += dppf<ROR4>(y[c]);
    }
    LAS f32x4* qp = (LAS f32x4*)(L + L_QP + w * 512 + (lane >> 4) * 128 + (lane & 3) * 32);
    qp[0] = f32x4{y[0], y[1], y[2], y[3]};
    qp[1] = f32x4{y[4], y[5], y[6], y[7]};
  }
  *(LAS unsigned*)(L + (w == 0 ? L_ML : L_O)) = ep;
  LDS_BARRIER();
  const unsigned tag = __builtin_amdgcn_readfirstlane(*(volatile LAS unsigned*)(L + L_ML)) + 1u;
  if (w == 0) {
    const unsigned tt = ((NOW() >> 2) & 0xffu) | tag << 8;
    const LAS f32x4* qp = (const LAS f32x4*)(L + L_QP + (lane & 31) * 16);
    const f32x4 q = (qp[0] + qp[32]) + (qp[64] + qp[96]);
    const unsigned lo = cvtpk(q.x, q.y), hi = cvtpk(q.z, q.w);
    __builtin_amdgcn_raw_buffer_store_b128(v4u{pk_lo(lo, tt), __builtin_amdgcn_alignbit(tt + (TAG_INC << 8), lo, 16), pk_lo(hi, tt + (2 * TAG_INC << 8)),
                                               __builtin_amdgcn_alignbit(tt + (3 * TAG_INC << 8), hi, 16)},
                                           rws, lane < 32 ? voff : OOB, WS_QG + (kr * 32 + br * 16 + j) * 512, AUX_SC1);
  }
  SB();
  ISSUE(H_A + 4, H_P)
  int budget;
  {
    const int qg_off = WS_QG + kvh * 32 * 512 + b * 128, qg_v = (tid >> 3) * 512 + (tid & 7) * 16;
    const int ck_v = w == 0 ? (lane & 15) * (16 * 512) + 2 * 512 : OOB;
    const unsigned ck_mask = w == 0 ? ~0u : 0u;
    v4u qg = __builtin_amdgcn_raw_buffer_load_b128(rws, qg_v, qg_off, AUX_SC1);
    v4u ck = __builtin_amdgcn_raw_buffer_load_b128(rws, ck_v, WS_QG, AUX_SC1);
    SB();
    ISSUE(H_P, H_C)
    KEEP(cpf);
    KEEP(tch);
    if (__any((diff4(qg, tag) | (diff4(ck, tag) & ck_mask)) >> 24)) {
#pragma unroll 1
      for (unsigned spins = 0; spins < SPIN_CAP; ++spins) {
        MEMFENCE();
        qg = __builtin_amdgcn_raw_buffer_load_b128(rws, qg_v, qg_off, AUX_SC1);
        ck = __builtin_amdgcn_raw_buffer_load_b128(rws, ck_v, WS_QG, AUX_SC1);
        if (!__any((diff4(qg, tag) | (diff4(ck, tag) & ck_mask)) >> 24)) break;
        __builtin_amdgcn_s_sleep(2);
      }
    }
    *(LAS v2u*)(L + L_Q + tid * 8) = v2u{pk_lo(qg.x, qg.y), pk_lo(qg.z, qg.w)};
    const unsigned ref = (t0 + PUB_NOM) & ~3u;
    const float d = (float)(int)(signed char)((ck.x >> 16) - (ref >> 2));
    const int mean = (int)row_sum(d) >> 2, skew = (int)row_max(d) * 4 - mean;
    *(LAS v4u*)(L + (w == 0 ? L_SLIP : L_O)) = v4u{0u, ref + (unsigned)mean - PUB_NOM, (unsigned)skew, 0u};
  }
  LDS_BARRIER();
  {
    const v4u an = *(const LAS v4u*)(L + L_SLIP);
    t0 = __builtin_amdgcn_readfirstlane(t0 + min(max((int)(an.y - t0), -150), 400));
    budget = __builtin_amdgcn_readfirstlane(EARLY + ((min(max((int)an.z - SK_0, 0), 200) * SK_K) >> 2));
  }
  v4u q0 = *(const LAS v4u*)(L + L_Q + (lane & 31) * 16), q1 = *(const LAS v4u*)(L + L_Q + 512 + (lane & 31) * 16);

  float acc[2][8], m = -INFINITY, l = 0.f;
#pragma unroll
  for (int k = 0; k < 8; ++k) acc[0][k] = acc[1][k] = 0.f;
#define QK_STEP(KR, i)                                                 \
  {                                                                    \
    float s0 = dot2(KR[i][0], q0[0], 0.f), s1 = dot2(KR[i][0], q1[0], 0.f); \
    s0 = dot2(KR[i][1], q0[1], s0); s1 = dot2(KR[i][1], q1[1], s1);    \
    s0 = dot2(KR[i][2], q0[2], s0); s1 = dot2(KR[i][2], q1[2], s1);    \
    s0 = dot2(KR[i][3], q0[3], s0); s1 = dot2(KR[i][3], q1[3], s1);    \
    y[i] = sum16(s0, s1);                                              \
  }
#define TR_STEP(H, CTRL, BIT)                                          \
  _Pragma("unroll") for (int i = 0; i < (H); ++i) {                    \
    const float keep = (BIT) ? y[i + (H)] : y[i], send = (BIT) ? y[i] : y[i + (H)]; \
    y[i] = keep + dppf<CTRL>(send);                                    \
  }
#define SCORES(slot)                                                   \
  TR_STEP(8, ROR8, b3) TR_STEP(4, XOR7, b2) TR_STEP(2, XOR2, b1) TR_STEP(1, XOR1, b0) \
  m = fmaxf(m, y[0]);                                                  \
  sc[(slot) * 256] = y[0];
#define PROBS(slot)                                                                     \
  const float pp = __builtin_amdgcn_exp2f((sc[(slot) * 256] - m) * SCL);                \
  l += pp;                                                                              \
  const unsigned ppk = cvtpk(pp, dppf<XOR1>(pp));        \
  const auto ph = __builtin_amdgcn_permlane16_swap(ppk, ppk, false, false);
#define PV_STEP(t)                                                     \
  {                                                                    \
    const unsigned p0 = dppu<BCAST + 2 * (t)>(ph[0]), p1 = dppu<BCAST + 2 * (t)>(ph[1]); \
    _Pragma("unroll") for (int k = 0; k < 4; ++k) {                    \
      const unsigned lo = pk_lo(R[2 * (t)][k], R[2 * (t) + 1][k]), hi = pk_hi(R[2 * (t)][k], R[2 * (t) + 1][k]); \
      acc[0][2 * k] = dot2(lo, p0, acc[0][2 * k]); acc[0][2 * k + 1] = dot2(hi, p0, acc[0][2 * k + 1]); \
      acc[1][2 * k] = dot2(lo, p1, acc[1][2 * k]); acc[1][2 * k + 1] = dot2(hi, p1, acc[1][2 * k + 1]); \
    }                                                                  \
  }
#define DUMP(brn)                                                      \
  {                                                                    \
    float o[8];                                                        \
    _Pragma("unroll") for (int k = 0; k < 8; ++k) o[k] = sum32(acc[0][k], acc[1][k]);     \
    LAS f32x4* mp = (LAS f32x4*)(L + L_MRG + (brn) * 8192 + tid * 32); \
    mp[0] = f32x4{o[0], o[1], o[2], o[3]};                             \
    mp[1] = f32x4{o[4], o[5], o[6], o[7]};                             \
    l = row_sum(l);                                                    \
    l = sum32(l, l);                                                   \
    *(LAS f32x2*)(L + L_ML + (brn) * 64 + w * 16 + ((lane >> 4) & 1) * 8) = f32x2{m, l};       \
  }
  {
    float y[16];
#pragma unroll
    for (int i = 0; i < 16; ++i) {
      if (i % H_S == 0) { SB(); ISSUE(H_C + (i / H_S) * 4, H_C + (i / H_S) * 4 + 4) }
      QK_STEP(X0, i)
    }
    SCORES(0)
#pragma unroll
    for (int i = 0; i < 16; ++i) {
      if ((i + 16) % H_S == 0) { SB(); ISSUE(H_C + ((i + 16) / H_S) * 4, H_C + ((i + 16) / H_S) * 4 + 4) }
      QK_STEP(X1, i)
    }
    SCORES(1)
    SB();
  }
  ISSUE(H_C + (31 / H_S + 1) * 4, 48)
  v4u WO[8];
  const int ooff = (lane & 15) < 14 ? (lane >> 4) * (DM * 2) + (lane & 15) * 16 : OOB;
#define WO_LOADS(lo, hi)                                                                  \
  SB();                                                                                   \
  _Pragma("unroll") for (int n = (lo); n < (hi); ++n)                                     \
    WO[n] = __builtin_amdgcn_raw_buffer_load_b128(__builtin_amdgcn_make_buffer_rsrc((void*)wop, 0, 127 * DM * 2 + 224, RSRC_W3), ooff, \
                                                  (32 * w + 4 * n) * (DM * 2), AUX_NT);   \
  SB();
  wait_until(t0 + ((48 * PACE_PER + poff) >> 8));
  WO_LOADS(0, WO_E)
  t0 += WO_T;
  {
    const unsigned slip_addr = (unsigned)(unsigned long)(L + L_SLIP);
    int lprev = 1 << 20;
    int xt = 48 * PACE_PER + poff;
#define BURST(g)                                                       \
  { const int inc_ = min(budget, EARLY_STEP); t0 += inc_; budget -= inc_; } \
  pace(t0 + (min(xt, PACE_REL * PACE_PER + PACE_OFF * 256) >> 8), lprev, slip_addr); \
  xt += 4 * PACE_PER;                                                  \
  _Pragma("unroll") for (int u_ = 4 * (g); u_ < 4 * (g) + 4; ++u_) { R[u_] = LDU(rn, u_); SB(); }
    const char *kp = (const char*)p.k0 + reg_off, *vp = (const char*)p.v0 + reg_off;
    int g = 2;
#pragma unroll 1
    for (int grp = 0;; ++grp) {
#pragma unroll 1
      do {
        const auto rn = __builtin_amdgcn_make_buffer_rsrc((void*)(g < 7 ? kp + (g + 1) * 65536 : vp), 0, 65536, RSRC_W3);
        float y[16];
#pragma unroll
        for (int i = 0; i < 16; ++i) {
          QK_STEP(R, i)
          if ((i & 3) == 3) { BURST(i >> 2) }
        }
        SCORES(g)
        SB();
      } while (++g < 8);
      m = max32(row_max(m));
      kp = (const char*)p.k1 + reg_off;
      budget += GAP_T;
      g = 0;
#pragma unroll 1
      do {
        const auto rn = __builtin_amdgcn_make_buffer_rsrc((void*)(g < 7 ? vp + (g + 1) * 65536 : kp), 0, 65536, RSRC_W3);
        PROBS(g)
        PV_STEP(0) PV_STEP(1) BURST(0) PV_STEP(2) PV_STEP(3) BURST(1) PV_STEP(4) PV_STEP(5) BURST(2) PV_STEP(6) PV_STEP(7) BURST(3)
      } while (++g < 8 - grp);
      if (grp) break;
      DUMP(0)
#pragma unroll
      for (int k = 0; k < 8; ++k) acc[0][k] = acc[1][k] = 0.f;
      m = -INFINITY;
      l = 0.f;
      q0 = *(const LAS v4u*)(L + L_Q + 1024 + (lane & 31) * 16);
      q1 = *(const LAS v4u*)(L + L_Q + 1536 + (lane & 31) * 16);
      vp = (const char*)p.v1 + reg_off;
      budget += GAP_T;
      g = 0;
    }
  }
  const unsigned t_end = t0 + __builtin_amdgcn_readfirstlane(*(volatile LAS unsigned*)(L + L_SLIP)) + ((512 * PACE_PER + PACE_OFF * 256) >> 8);
  {
    PROBS(7)
#define WO_AFTER(t) if (WO_A1 == (t)) { WO_LOADS(WO_E, (WO_E + 9) / 2) } if (WO_A2 == (t)) { WO_LOADS((WO_E + 9) / 2, 8) }
    PV_STEP(0) PV_STEP(1) PV_STEP(2) PV_STEP(3) WO_AFTER(3) PV_STEP(4) WO_AFTER(4) PV_STEP(5) WO_AFTER(5) PV_STEP(6) WO_AFTER(6) PV_STEP(7) WO_AFTER(7)
  }

  DUMP(1)
  WO_AFTER(8)
  LDS_BARRIER();
  WO_AFTER(9)
  {
    f32x2 ml[4];
    f32x4 ov[4];
#pragma unroll
    for (int i = 0; i < 4; ++i) {
      ml[i] = *(const LAS f32x2*)(L + L_ML + (w >> 1) * 64 + i * 16 + (w & 1) * 8);
      ov[i] = *(const LAS f32x4*)(L + L_MRG + (w >> 1) * 8192 + i * 2048 + (w & 1) * 1024 + voff);
    }
    const float M = fmaxf(fmaxf(ml[0].x, ml[1].x), fmaxf(ml[2].x, ml[3].x));
    f32x4 o = {0.f, 0.f, 0.f, 0.f};
    float ls = 0.f;
#pragma unroll
    for (int i = 0; i < 4; ++i) {
      const float e = __builtin_amdgcn_exp2f((ml[i].x - M) * SCL);
      o += e * ov[i];
      ls = fmaf(e, ml[i].y, ls);
    }
    const int og = WS_OG + ((w >> 1) * 256 + bid) * OG_STR;
    __builtin_amdgcn_raw_buffer_store_b128(enc4(o, tag), rws, voff, og + (w & 1) * 1024, AUX_SC1);
    __builtin_amdgcn_raw_buffer_store_b128(v4u{enc_f(M, tag), (__float_as_uint(M) & 0xffu) | tag << 8, enc_f(ls, tag), tag}, rws, lane == 0 ? 0 : OOB, og + 2048 + (w & 1) * 16, AUX_SC1);
  }
  WO_AFTER(10)
  wait_until(t_end + T_GATHER);

  {
    v4u g[4], gm;
    const int og_off = WS_OG + (br * 256 + kr * 32 + w * 8) * OG_STR, og_v = (lane >> 5) * OG_STR + (lane & 31) * 16;
    const int ml_v = (((lane & 3) * 2 + (lane >> 5)) ^ ((lane >> 2) & 1)) * OG_STR;
#define G_BAD() ((((gm.x ^ (tag << 24)) | (gm.z ^ (tag << 24)) | diff4(g[0], tag) | diff4(g[1], tag) | diff4(g[2], tag) | diff4(g[3], tag)) >> 24) | \
                 ((gm.y ^ (tag << 8)) >> 8) | (gm.w ^ tag))
#define GATHER()                                                                                          \
  gm = __builtin_amdgcn_raw_buffer_load_b128(rws, ml_v, og_off + 2048 + (rb >> 1) * 16, AUX_SC1);         \
  _Pragma("unroll") for (int s = 0; s < 4; ++s) g[s] = __builtin_amdgcn_raw_buffer_load_b128(rws, og_v, og_off + rb * 512 + s * 2 * OG_STR, AUX_SC1);
    GATHER()
    SB();
#pragma unroll
    for (int n = 0; n < 8; ++n) *(LAS v4u*)(L + L_WO + (32 * w + 4 * n + (lane >> 4)) * WO_STR + (lane & 15) * 16) = WO[n];
    SB();
    if (__any(G_BAD())) {
#pragma unroll 1
      for (unsigned spins = 0; spins < SPIN_CAP; ++spins) {
        MEMFENCE();
        GATHER()
        if (!__any(G_BAD())) break;
        __builtin_amdgcn_s_sleep(1);
      }
    }
    const float Ms = __uint_as_float(gm.x << 8 | (gm.y & 0xffu)), Ls = dec_f(gm.z);
    float Mx = fmaxf(Ms, dppf<XOR1>(Ms));
    Mx = fmaxf(Mx, dppf<XOR2>(Mx));
    Mx = fmaxf(Mx, dppf<XOR7>(Mx));
    const float e = __builtin_amdgcn_exp2f((Ms - Mx) * SCL);
    float Lt = e * Ls;
    Lt += dppf<XOR1>(Lt);
    Lt += dppf<XOR2>(Lt);
    Lt += dppf<XOR7>(Lt);
    const float c = e * __builtin_amdgcn_rcpf(Lt);
    float o0 = 0.f, o1 = 0.f, o2 = 0.f, o3 = 0.f;
#define MRG(s) { const float cs = dppf<BCAST + s>(c); o0 = fmaf(cs, dec_f(g[s].x), o0); o1 = fmaf(cs, dec_f(g[s].y), o1); o2 = fmaf(cs, dec_f(g[s].z), o2); o3 = fmaf(cs, dec_f(g[s].w), o3); }
    MRG(0) MRG(1) MRG(2) MRG(3)
    *(LAS unsigned*)(L + L_O + w * 256 + (lane & 31) * 8 + (lane >> 5) * 4) = cvtpk(sum32(o0, o2), sum32(o1, o3));
  }
  LDS_BARRIER();
  {
    v4u BW[2][4];
#pragma unroll
    for (int ks = 0; ks < 4; ++ks)
#pragma unroll
      for (int c = 0; c < 2; ++c) {
        LAS unsigned char* a = L + L_WO + (32 * ks + 8 * (lane >> 4) + ((lane >> 2) & 3)) * WO_STR + (2 * w + c) * 32 + (lane & 3) * 8;
        const v2u r0 = __builtin_bit_cast(v2u, __builtin_amdgcn_ds_read_tr16_b64_v4i16((LAS v4s*)a));
        const v2u r1 = __builtin_bit_cast(v2u, __builtin_amdgcn_ds_read_tr16_b64_v4i16((LAS v4s*)(a + 4 * WO_STR)));
        BW[c][ks] = v4u{r0.x, r0.y, r1.x, r1.y};
      }
    f32x4 d0 = {0.f, 0.f, 0.f, 0.f}, d1 = {0.f, 0.f, 0.f, 0.f};
#pragma unroll
    for (int ks = 0; ks < 4; ++ks) {
      const v4u o = *(const LAS v4u*)(L + L_O + (lane & 3) * 256 + ks * 64 + (lane >> 4) * 16);
      d0 = __builtin_amdgcn_mfma_f32_16x16x32_bf16(__builtin_bit_cast(bf16x8, BW[0][ks]), __builtin_bit_cast(bf16x8, o), d0, 0, 0, 0);
      d1 = __builtin_amdgcn_mfma_f32_16x16x32_bf16(__builtin_bit_cast(bf16x8, BW[1][ks]), __builtin_bit_cast(bf16x8, o), d1, 0, 0, 0);
    }
    const f32x4 d = b2 ? d1 : d0;
    __builtin_amdgcn_raw_buffer_store_b128(enc4(d, tag), rws,
                                           b3 ? OOB : (lane & 3) * 4096 + (lane & 4) * 16 + (lane >> 4) * 16,
                                           WS_PG + ((br * 4 + cb) * 4 + w) * 16384 + (kr * 4 + rb) * 128, AUX_SC1);
  }

  if (w == 3 && (bid & 1) == 0) {
    const int ri = bid >> 1;
    v4u g[4];
    DRAIN();
    wait_until(t_end + T_REDUCE);
#pragma unroll 1
    for (unsigned spins = 0; spins < SPIN_CAP; ++spins) {
      MEMFENCE();
#pragma unroll
      for (int s = 0; s < 4; ++s) g[s] = __builtin_amdgcn_raw_buffer_load_b128(rws, voff + s * 1024, WS_PG + ri * 4096, AUX_SC1);
      if (!__any((diff4(g[0], tag) | diff4(g[1], tag) | diff4(g[2], tag) | diff4(g[3], tag)) >> 24)) break;
      __builtin_amdgcn_s_sleep(1);
    }
    float sum[4];
#pragma unroll
    for (int k = 0; k < 4; ++k) {
      sum[k] = (dec_f(g[0][k]) + dec_f(g[1][k])) + (dec_f(g[2][k]) + dec_f(g[3][k]));
      sum[k] += dppf<ROR8>(sum[k]);
      sum[k] = sum16(sum[k], sum[k]);
      sum[k] = sum32(sum[k], sum[k]);
    }
    const auto ro = __builtin_amdgcn_make_buffer_rsrc(p.out, 0, 4 * 2 * DM * 2, RSRC_W3);
    __builtin_amdgcn_raw_buffer_store_b64(v2u{cvtpk(sum[0], sum[1]), cvtpk(sum[2], sum[3])}, ro, lane < ((ri & 12) == 12 ? 4 : 8) ? lane * 8 : OOB,
                                          ((ri & 3) * 2 * DM + (ri >> 6) * DM + ((ri >> 4) & 3) * 112 + ((ri >> 2) & 3) * 32) * 2, AUX_SC1);
    __builtin_amdgcn_raw_buffer_store_b32(tag, rws, (bid == 0 && lane == 0) ? 0 : OOB, WS_EPOCH, AUX_SC1);
  }
  asm volatile(".Ldec_code_end:");
}

}

extern "C" void kernel_launch(void* const* d_in, const int* in_sizes, int n_in, void* d_out, int out_size, void* d_ws, size_t ws_size,
                              hipStream_t stream) {
  Params p;
  p.x0 = d_in[0]; p.k0 = d_in[1]; p.v0 = d_in[2]; p.wq0 = d_in[3]; p.wo0 = d_in[4];
  p.x1 = d_in[5]; p.k1 = d_in[6]; p.v1 = d_in[7]; p.wq1 = d_in[8]; p.wo1 = d_in[9];
  p.out = d_out;
  p.ws = (unsigned char*)d_ws;
  decode_fused<<<256, 256, 0, stream>>>(p);
}
```

```cpp
#include <hip/hip_runtime.h>
#include <hip/hip_bf16.h>
#include <stdint.h>

namespace {

typedef unsigned v4u __attribute__((ext_vector_type(4)));
typedef unsigned v2u __attribute__((ext_vector_type(2)));
typedef unsigned long long v8p __attribute__((ext_vector_type(8)));
typedef unsigned long long v4p __attribute__((ext_vector_type(4)));
typedef float f32x4 __attribute__((ext_vector_type(4)));
typedef float f32x2 __attribute__((ext_vector_type(2)));
typedef short v4s __attribute__((ext_vector_type(4)));
typedef __bf16 bf16x2 __attribute__((ext_vector_type(2)));
typedef __bf16 bf16x8 __attribute__((ext_vector_type(8)));
#define LAS __attribute__((address_space(3)))
#define SB() __builtin_amdgcn_sched_barrier(0)
#define MEMFENCE() asm volatile("" ::: "memory")
#define KEEP(x) asm volatile("" :: "v"(x))
#define DRAIN() asm volatile("s_waitcnt vmcnt(0)" ::: "memory")
#define LDS_BARRIER() asm volatile("s_waitcnt lgkmcnt(0)\n\ts_barrier" ::: "memory")
#define NOW() ((unsigned)__builtin_amdgcn_s_memrealtime())

constexpr int DM = 448, NQ = 4096, SK = 8192, DH = 256;
constexpr unsigned REGION_BYTES = SK * DH * 2;

constexpr int PACE_GBS = 7810;
constexpr int PACE_OFF = 145;
constexpr int PACE_REL = 510;
constexpr int PUB_NOM = 200;
constexpr int EARLY = 36;
constexpr int EARLY_STEP = 3;
constexpr int SK_0 = 45;
constexpr int SK_K = 2;
constexpr int GAP_T = 4;
constexpr int SLIP_THR = 90;
constexpr int SLIP_N = 5;
constexpr int H_A = 8;
constexpr int H_P = 24;
constexpr int H_C = 32;
constexpr int H_S = 11;
constexpr int WO_A1 = 7;
constexpr int WO_A2 = 9;
constexpr int WO_E = 2;
constexpr int WO_T = (WO_E * 13);
constexpr int T_GATHER = 150;
constexpr int T_REDUCE = 340;
constexpr int PACE_PER = (int)(1048576.0 * 256.0 / (PACE_GBS * 10.0) + 0.5);
static_assert(H_C >= 32 && H_C <= 44 && H_P <= H_C && H_A % 4 == 0 && H_P % 4 == 0 && H_C % 4 == 0, "head schedule");

constexpr int AUX_NT = 2, AUX_SC1 = 16, RSRC_W3 = 0x00020000;
constexpr int OOB = 0x7fff0000;
constexpr unsigned SPIN_CAP = 1u << 18;
constexpr float SCL = 0.0625f * 1.4426950408889634f;

constexpr unsigned WS_EPOCH = 0;
constexpr unsigned WS_QG = 32768;
constexpr unsigned OG_STR = 2048 + 128;
constexpr unsigned WS_OG = WS_QG + 256 * 512;
constexpr unsigned WS_PG = WS_OG + 2 * 256 * OG_STR;
constexpr unsigned WS_END = WS_PG + 128 * 4096;

constexpr int WO_STR = 288;
constexpr int L_WO = 0;
constexpr int L_MRG = 128 * WO_STR;
constexpr int L_QP = L_MRG + 16384;
constexpr int L_Q = L_QP + 2048;
constexpr int L_O = L_Q + 2048;
constexpr int L_ML = L_O + 1024;
constexpr int L_SLIP = L_ML + 128;
constexpr int L_SC = L_SLIP + 16;
constexpr int L_BYTES = 84 * 1024;
static_assert(L_SC + 8 * 1024 <= L_BYTES && L_SC % 16 == 0, "lds map");

struct Params {
  const void *x0, *k0, *v0, *wq0, *wo0, *x1, *k1, *v1, *wq1, *wo1;
  void* out;
  unsigned char* ws;
};

__device__ __forceinline__ float dot2(unsigned a, unsigned b, float c) {
  return __builtin_amdgcn_fdot2_f32_bf16(__builtin_bit_cast(bf16x2, a), __builtin_bit_cast(bf16x2, b), c, false);
}
__device__ __forceinline__ unsigned pk_lo(unsigned a, unsigned b) { return __builtin_amdgcn_perm(b, a, 0x05040100u); }
__device__ __forceinline__ unsigned pk_hi(unsigned a, unsigned b) { return __builtin_amdgcn_perm(b, a, 0x07060302u); }
__device__ __forceinline__ unsigned cvtpk(float lo, float hi) {
  bf16x2 c = {(__bf16)lo, (__bf16)hi};
  return __builtin_bit_cast(unsigned, c);
}
template <int CTRL> __device__ __forceinline__ float dppf(float x) {
  return __builtin_bit_cast(float, __builtin_amdgcn_mov_dpp(__builtin_bit_cast(int, x), CTRL, 0xf, 0xf, true));
}
template <int CTRL> __device__ __forceinline__ unsigned dppu(unsigned x) {
  return (unsigned)__builtin_amdgcn_mov_dpp((int)x, CTRL, 0xf, 0xf, true);
}
constexpr int XOR1 = 0xB1, XOR2 = 0x4E, XOR7 = 0x141, ROR8 = 0x128, ROR4 = 0x124, ROR2 = 0x122, ROR1 = 0x121, BCAST = 0x150;
__device__ __forceinline__ float row_sum(float a) {
  a += dppf<ROR8>(a); a += dppf<ROR4>(a); a += dppf<ROR2>(a);
  return a + dppf<ROR1>(a);
}
__device__ __forceinline__ float row_max(float a) {
  a = fmaxf(a, dppf<ROR8>(a)); a = fmaxf(a, dppf<ROR4>(a)); a = fmaxf(a, dppf<ROR2>(a));
  return fmaxf(a, dppf<ROR1>(a));
}
__device__ __forceinline__ float sum16(float a, float b) {
  auto r = __builtin_amdgcn_permlane16_swap(__float_as_uint(a), __float_as_uint(b), false, false);
  return __uint_as_float(r[0]) + __uint_as_float(r[1]);
}
__device__ __forceinline__ float sum32(float a, float b) {
  auto r = __builtin_amdgcn_permlane32_swap(__float_as_uint(a), __float_as_uint(b), false, false);
  return __uint_as_float(r[0]) + __uint_as_float(r[1]);
}
__device__ __forceinline__ float max32(float a) {
  auto r = __builtin_amdgcn_permlane32_swap(__float_as_uint(a), __float_as_uint(a), false, false);
  return fmaxf(__uint_as_float(r[0]), __uint_as_float(r[1]));
}
constexpr unsigned TAG_INC = 0x35;
__device__ __forceinline__ unsigned enc_f(float f, unsigned tag) { return __builtin_amdgcn_alignbit(tag, __float_as_uint(f), 8); }
__device__ __forceinline__ v4u enc4(f32x4 f, unsigned tag) {
  return v4u{enc_f(f.x, tag), enc_f(f.y, tag + TAG_INC), enc_f(f.z, tag + 2 * TAG_INC), enc_f(f.w, tag + 3 * TAG_INC)};
}
__device__ __forceinline__ float dec_f(unsigned d) { return __uint_as_float(d << 8); }
__device__ __forceinline__ unsigned diff4(v4u g, unsigned tag) {
  return ((g.x ^ (tag << 24)) | (g.y ^ ((tag + TAG_INC) << 24))) | ((g.z ^ ((tag + 2 * TAG_INC) << 24)) | (g.w ^ ((tag + 3 * TAG_INC) << 24)));
}

__device__ __forceinline__ void wait_until(unsigned t) {
  SB();
  asm volatile("1:\n\ts_memrealtime vcc\n\ts_waitcnt lgkmcnt(0)\n\ts_sub_u32 vcc_lo, vcc_lo, %0\n\ts_cmp_lt_i32 vcc_lo, 0\n\t"
               "s_cbranch_scc0 2f\n\ts_sleep 1\n\ts_branch 1b\n2:" :: "s"(__builtin_amdgcn_readfirstlane(t)) : "vcc", "scc", "memory");
  SB();
}
__device__ __forceinline__ void pace(unsigned t, int& lprev, unsigned slip_addr) {
  unsigned vt, slip, tgt;
  int lp = __builtin_amdgcn_readfirstlane(lprev);
  SB();
  asm volatile(
      "ds_read_b32 %0, %5\n\ts_memrealtime vcc\n\ts_waitcnt lgkmcnt(0)\n\t"
      "v_readfirstlane_b32 %1, %0\n\t"
      "s_add_u32 %2, %1, %4\n\t"
      "s_sub_u32 vcc_lo, vcc_lo, %2\n\t"
      "s_lshl_b32 %3, %3, 4\n\ts_sub_i32 vcc_hi, vcc_lo, %3\n\t"
      "s_cmp_gt_i32 vcc_hi, %7\n\t"
      "s_cbranch_scc0 3f\n\t"
      "s_add_u32 %1, %1, %6\n\ts_add_u32 %2, %2, %6\n\ts_sub_u32 vcc_lo, vcc_lo, %6\n\t"
      "v_mov_b32 %0, %1\n\tds_max_u32 %5, %0\n"
      "3:\n\ts_max_i32 %3, vcc_lo, 0\n"
      "1:\n\ts_cmp_lt_i32 vcc_lo, 0\n\ts_cbranch_scc0 2f\n\ts_sleep 1\n\t"
      "s_memrealtime vcc\n\ts_waitcnt lgkmcnt(0)\n\ts_sub_u32 vcc_lo, vcc_lo, %2\n\ts_branch 1b\n"
      "2:\n\ts_waitcnt lgkmcnt(0)"
      : "=&v"(vt), "=&s"(slip), "=&s"(tgt), "+s"(lp)
      : "s"(__builtin_amdgcn_readfirstlane(t)), "v"(slip_addr), "i"((SLIP_N * 4 * PACE_PER + 128) >> 8), "i"(SLIP_THR)
      : "vcc", "scc", "memory");
  lprev = __builtin_amdgcn_readfirstlane(lp);
  SB();
}


__global__ void __launch_bounds__(256, 1) decode_fused(Params args) {
  __shared__ __attribute__((aligned(16))) unsigned char smem[L_BYTES];
  LAS unsigned char* L = (LAS unsigned char*)smem;
  v8p arg_a;
  v4p arg_b;
  unsigned long long clk0;
  asm volatile("s_load_dwordx16 %0, %3, 0x0\n\ts_load_dwordx8 %1, %3, 0x40\n\ts_memrealtime %2"
               : "=&s"(arg_a), "=&s"(arg_b), "=&s"(clk0) : "s"(__builtin_amdgcn_kernarg_segment_ptr()));
  SB();
  const int bid = blockIdx.x, kvh = bid >> 5, b = (bid >> 3) & 3, sp = bid & 7;
  const int br = bid >> 7, j = (bid >> 3) & 15, kr = (sp + 4 * br) & 7, cb = j & 3, rb = j >> 2;
  const int tid = threadIdx.x, lane = tid & 63, w = __builtin_amdgcn_readfirstlane(tid >> 6);
  const int voff = lane * 16, koff = tid * 16;
  const bool b3 = lane & 8, b2 = lane & 4, b1 = lane & 2, b0 = lane & 1;
  LAS float* const sc = (LAS float*)(L + L_SC + tid * 4);

  unsigned cpf;
  {
    unsigned long long pc;
    unsigned len;
    asm volatile("s_getpc_b64 %0\n.Ldec_code_begin:\n\ts_mov_b32 %1, .Ldec_code_end-.Ldec_code_begin" : "=s"(pc), "=s"(len));
    const auto rc = __builtin_amdgcn_make_buffer_rsrc((void*)pc, 0, len, RSRC_W3);
    cpf = __builtin_amdgcn_raw_buffer_load_b32(rc, tid < 8 ? tid * 128 : OOB, (bid >> 3) * 1024, 0);
  }
  const int row0 = 128 * w + 8 * (lane >> 2);
  int xoff = row0 < DM ? row0 * 2 : OOB, woff = row0 * (NQ * 2) + (lane & 3) * 16, wso = (kr * 512 + j * 32) * 2;
  int poffv = w == 3 && lane < 56 ? (lane >> 1) * (16 * NQ * 2) + (lane & 1) * 64 + kr * 1024 + j * (NQ * 2) : OOB;
  int eso = w == 0 ? WS_EPOCH : OOB;
  asm volatile("" : "+v"(xoff), "+v"(woff), "+v"(poffv));
  SB();
  asm volatile("s_waitcnt lgkmcnt(0)" : "+s"(arg_a), "+s"(arg_b), "+s"(clk0), "+s"(wso), "+s"(eso));
  SB();
  Params p;
  p.x0 = (void*)arg_a[0]; p.k0 = (void*)arg_a[1]; p.v0 = (void*)arg_a[2]; p.wq0 = (void*)arg_a[3]; p.wo0 = (void*)arg_a[4];
  p.x1 = (void*)arg_a[5]; p.k1 = (void*)arg_a[6]; p.v1 = (void*)arg_a[7]; p.wq1 = (void*)arg_b[0]; p.wo1 = (void*)arg_b[1];
  p.out = (void*)arg_b[2]; p.ws = (unsigned char*)arg_b[3];
  unsigned t0 = (unsigned)clk0;
  const auto rws = __builtin_amdgcn_make_buffer_rsrc(p.ws, 0, WS_END, RSRC_W3);

  v4u X[4], W[8], X0[16], X1[16], R[16];
  unsigned pfw;
  unsigned ep;
  {
    const auto rx = __builtin_amdgcn_make_buffer_rsrc((void*)(br ? p.x1 : p.x0), 0, 4 * DM * 2, RSRC_W3);
    const auto rwq = __builtin_amdgcn_make_buffer_rsrc((void*)(br ? p.wq1 : p.wq0), 0, DM * NQ * 2, RSRC_W3);
#pragma unroll
    for (int i = 0; i < 4; ++i) X[i] = __builtin_amdgcn_raw_buffer_load_b128(rx, xoff + i * DM * 2, 0, 0);
    ep = __builtin_amdgcn_raw_buffer_load_b32(rws, 0, eso, AUX_SC1);
    pfw = __builtin_amdgcn_raw_buffer_load_b32(rwq, poffv, (384u - (unsigned)(size_t)(br ? p.wq1 : p.wq0)) & 896u, AUX_NT);
    SB();
#pragma unroll
    for (int n = 0; n < 8; ++n) W[n] = __builtin_amdgcn_raw_buffer_load_b128(rwq, woff, wso + n * (NQ * 2), AUX_NT);
    SB();
  }
  asm volatile("" : "+s"(p.wo0), "+s"(p.wo1), "+s"(p.v1));
  const size_t reg_off = (size_t)(b * 8 + kvh) * REGION_BYTES + sp * (REGION_BYTES / 8);
  const char* const wop = (const char*)(br ? p.wo1 : p.wo0) + (kr * 512 + rb * 128) * (DM * 2) + cb * 224;
  unsigned tch;
  {
    const size_t a12 = w == 1 ? (size_t)p.v0 : (size_t)p.k1, a03 = w == 3 ? (size_t)p.v1 : (size_t)wop;
    const size_t a = (w == 1 || w == 2 ? a12 : a03) + (w ? reg_off : 0);
    tch = __builtin_amdgcn_raw_buffer_load_b32(__builtin_amdgcn_make_buffer_rsrc((void*)a, 0, 65536, RSRC_W3), lane == 0 ? 0 : OOB, ((bid * 3 + w) & 63) * 896, AUX_NT);
    SB();
  }
#define LDU(rs, i) __builtin_amdgcn_raw_buffer_load_b128(rs, koff, (i) * 4096, AUX_NT)
  const int poff = PACE_OFF * 256 + (((b * 8 + kvh) * 19) & 31) * (4 * PACE_PER / 32);
  const auto rk0 = __builtin_amdgcn_make_buffer_rsrc((char*)p.k0 + reg_off, 0, 48 * 4096, RSRC_W3);
#define ISSUE(lo, hi)                                                                     \
  _Pragma("unroll") for (int i_ = (lo); i_ < (hi) && i_ < 48; ++i_) {                     \
    if (i_ >= H_A + 4 && (i_ & 3) == 0) wait_until(t0 + ((i_ * PACE_PER + poff) >> 8));   \
    if (i_ < 16) X0[i_ & 15] = LDU(rk0, i_);                                              \
    else if (i_ < 32) X1[i_ & 15] = LDU(rk0, i_);                                         \
    else R[i_ & 15] = LDU(rk0, i_);                                                       \
    SB();                                                                                 \
  }
  ISSUE(0, H_A)

  {
    float a[4][8];
#pragma unroll
    for (int bb = 0; bb < 4; ++bb)
#pragma unroll
      for (int c = 0; c < 8; ++c) a[bb][c] = 0.f;
#pragma unroll
    for (int t = 0; t < 4; ++t) {
#pragma unroll
      for (int k = 0; k < 4; ++k) {
        const unsigned lo = pk_lo(W[2 * t][k], W[2 * t + 1][k]), hi = pk_hi(W[2 * t][k], W[2 * t + 1][k]);
#pragma unroll
        for (int bb = 0; bb < 4; ++bb) {
          a[bb][2 * k] = dot2(lo, X[bb][t], a[bb][2 * k]);
          a[bb][2 * k + 1] = dot2(hi, X[bb][t], a[bb][2 * k + 1]);
        }
      }
      SB();
      ISSUE(H_A + t, H_A + t + 1)
    }
    float y[8];
#pragma unroll
    for (int c = 0; c < 8; ++c) {
      y[c] = sum16(sum32(a[0][c], a[2][c]), sum32(a[1][c], a[3][c]));
      y[c] += dppf<ROR8>(y[c]);
      y[c] += dppf<ROR4>(y[c]);
    }
    LAS f32x4* qp = (LAS f32x4*)(L + L_QP + w * 512 + (lane >> 4) * 128 + (lane & 3) * 32);
    qp[0] = f32x4{y[0], y[1], y[2], y[3]};
    qp[1] = f32x4{y[4], y[5], y[6], y[7]};
  }
  *(LAS unsigned*)(L + (w == 0 ? L_ML : L_O)) = ep;
  LDS_BARRIER();
  const unsigned tag = __builtin_amdgcn_readfirstlane(*(volatile LAS unsigned*)(L + L_ML)) + 1u;
  if (w == 0) {
    const unsigned tt = ((NOW() >> 2) & 0xffu) | tag << 8;
    const LAS f32x4* qp = (const LAS f32x4*)(L + L_QP + (lane & 31) * 16);
    const f32x4 q = (qp[0] + qp[32]) + (qp[64] + qp[96]);
    const unsigned lo = cvtpk(q.x, q.y), hi = cvtpk(q.z, q.w);
    __builtin_amdgcn_raw_buffer_store_b128(v4u{pk_lo(lo, tt), __builtin_amdgcn_alignbit(tt + (TAG_INC << 8), lo, 16), pk_lo(hi, tt + (2 * TAG_INC << 8)),
                                               __builtin_amdgcn_alignbit(tt + (3 * TAG_INC << 8), hi, 16)},
                                           rws, lane < 32 ? voff : OOB, WS_QG + (kr * 32 + br * 16 + j) * 512, AUX_SC1);
  }
  SB();
  ISSUE(H_A + 4, H_P)
  int budget;
  {
    const int qg_off = WS_QG + kvh * 32 * 512 + b * 128, qg_v = (tid >> 3) * 512 + (tid & 7) * 16;
    const int ck_v = w == 0 ? (lane & 15) * (16 * 512) + 2 * 512 : OOB;
    const unsigned ck_mask = w == 0 ? ~0u : 0u;
    v4u qg = __builtin_amdgcn_raw_buffer_load_b128(rws, qg_v, qg_off, AUX_SC1);
    v4u ck = __builtin_amdgcn_raw_buffer_load_b128(rws, ck_v, WS_QG, AUX_SC1);
    SB();
    ISSUE(H_P, H_C)
    KEEP(cpf);
    KEEP(pfw);
    KEEP(tch);
    if (__any((diff4(qg, tag) | (diff4(ck, tag) & ck_mask)) >> 24)) {
#pragma unroll 1
      for (unsigned spins = 0; spins < SPIN_CAP; ++spins) {
        MEMFENCE();
        qg = __builtin_amdgcn_raw_buffer_load_b128(rws, qg_v, qg_off, AUX_SC1);
        ck = __builtin_amdgcn_raw_buffer_load_b128(rws, ck_v, WS_QG, AUX_SC1);
        if (!__any((diff4(qg, tag) | (diff4(ck, tag) & ck_mask)) >> 24)) break;
        __builtin_amdgcn_s_sleep(2);
      }
    }
    *(LAS v2u*)(L + L_Q + tid * 8) = v2u{pk_lo(qg.x, qg.y), pk_lo(qg.z, qg.w)};
    const unsigned ref = (t0 + PUB_NOM) & ~3u;
    const float d = (float)(int)(signed char)((ck.x >> 16) - (ref >> 2));
    const int mean = (int)row_sum(d) >> 2, skew = (int)row_max(d) * 4 - mean;
    *(LAS v4u*)(L + (w == 0 ? L_SLIP : L_O)) = v4u{0u, ref + (unsigned)mean - PUB_NOM, (unsigned)skew, 0u};
  }
  LDS_BARRIER();
  {
    const v4u an = *(const LAS v4u*)(L + L_SLIP);
    t0 = __builtin_amdgcn_readfirstlane(t0 + min(max((int)(an.y - t0), -150), 400));
    budget = __builtin_amdgcn_readfirstlane(EARLY + ((min(max((int)an.z - SK_0, 0), 200) * SK_K) >> 2));
  }
  v4u q0 = *(const LAS v4u*)(L + L_Q + (lane & 31) * 16), q1 = *(const LAS v4u*)(L + L_Q + 512 + (lane & 31) * 16);

  float acc[2][8], m = -INFINITY, l = 0.f;
#pragma unroll
  for (int k = 0; k < 8; ++k) acc[0][k] = acc[1][k] = 0.f;
#define QK_STEP(KR, i)                                                 \
  {                                                                    \
    float s0 = dot2(KR[i][0], q0[0], 0.f), s1 = dot2(KR[i][0], q1[0], 0.f); \
    s0 = dot2(KR[i][1], q0[1], s0); s1 = dot2(KR[i][1], q1[1], s1);    \
    s0 = dot2(KR[i][2], q0[2], s0); s1 = dot2(KR[i][2], q1[2], s1);    \
    s0 = dot2(KR[i][3], q0[3], s0); s1 = dot2(KR[i][3], q1[3], s1);    \
    y[i] = sum16(s0, s1);                                              \
  }
#define TR_STEP(H, CTRL, BIT)                                          \
  _Pragma("unroll") for (int i = 0; i < (H); ++i) {                    \
    const float keep = (BIT) ? y[i + (H)] : y[i], send = (BIT) ? y[i] : y[i + (H)]; \
    y[i] = keep + dppf<CTRL>(send);                                    \
  }
#define SCORES(slot)                                                   \
  TR_STEP(8, ROR8, b3) TR_STEP(4, XOR7, b2) TR_STEP(2, XOR2, b1) TR_STEP(1, XOR1, b0) \
  m = fmaxf(m, y[0]);                                                  \
  sc[(slot) * 256] = y[0];
#define PROBS(slot)                                                                     \
  const float pp = __builtin_amdgcn_exp2f((sc[(slot) * 256] - m) * SCL);                \
  l += pp;                                                                              \
  const unsigned ppk = cvtpk(pp, dppf<XOR1>(pp));        \
  const auto ph = __builtin_amdgcn_permlane16_swap(ppk, ppk, false, false);
#define PV_STEP(t)                                                     \
  {                                                                    \
    const unsigned p0 = dppu<BCAST + 2 * (t)>(ph[0]), p1 = dppu<BCAST + 2 * (t)>(ph[1]); \
    _Pragma("unroll") for (int k = 0; k < 4; ++k) {                    \
      const unsigned lo = pk_lo(R[2 * (t)][k], R[2 * (t) + 1][k]), hi = pk_hi(R[2 * (t)][k], R[2 * (t) + 1][k]); \
      acc[0][2 * k] = dot2(lo, p0, acc[0][2 * k]); acc[0][2 * k + 1] = dot2(hi, p0, acc[0][2 * k + 1]); \
      acc[1][2 * k] = dot2(lo, p1, acc[1][2 * k]); acc[1][2 * k + 1] = dot2(hi, p1, acc[1][2 * k + 1]); \
    }                                                                  \
  }
#define DUMP(brn)                                                      \
  {                                                                    \
    float o[8];                                                        \
    _Pragma("unroll") for (int k = 0; k < 8; ++k) o[k] = sum32(acc[0][k], acc[1][k]);     \
    LAS f32x4* mp = (LAS f32x4*)(L + L_MRG + (brn) * 8192 + tid * 32); \
    mp[0] = f32x4{o[0], o[1], o[2], o[3]};                             \
    mp[1] = f32x4{o[4], o[5], o[6], o[7]};                             \
    l = row_sum(l);                                                    \
    l = sum32(l, l);                                                   \
    *(LAS f32x2*)(L + L_ML + (brn) * 64 + w * 16 + ((lane >> 4) & 1) * 8) = f32x2{m, l};       \
  }
  {
    float y[16];
#pragma unroll
    for (int i = 0; i < 16; ++i) {
      if (i % H_S == 0) { SB(); ISSUE(H_C + (i / H_S) * 4, H_C + (i / H_S) * 4 + 4) }
      QK_STEP(X0, i)
    }
    SCORES(0)
#pragma unroll
    for (int i = 0; i < 16; ++i) {
      if ((i + 16) % H_S == 0) { SB(); ISSUE(H_C + ((i + 16) / H_S) * 4, H_C + ((i + 16) / H_S) * 4 + 4) }
      QK_STEP(X1, i)
    }
    SCORES(1)
    SB();
  }
  ISSUE(H_C + (31 / H_S + 1) * 4, 48)
  v4u WO[8];
  const int ooff = (lane & 15) < 14 ? (lane >> 4) * (DM * 2) + (lane & 15) * 16 : OOB;
#define WO_LOADS(lo, hi)                                                                  \
  SB();                                                                                   \
  _Pragma("unroll") for (int n = (lo); n < (hi); ++n)                                     \
    WO[n] = __builtin_amdgcn_raw_buffer_load_b128(__builtin_amdgcn_make_buffer_rsrc((void*)wop, 0, 127 * DM * 2 + 224, RSRC_W3), ooff, \
                                                  (32 * w + 4 * n) * (DM * 2), AUX_NT);   \
  SB();
  wait_until(t0 + ((48 * PACE_PER + poff) >> 8));
  WO_LOADS(0, WO_E)
  t0 += WO_T;
  {
    const unsigned slip_addr = (unsigned)(unsigned long)(L + L_SLIP);
    int lprev = 1 << 20;
    int xt = 48 * PACE_PER + poff;
#define BURST(g)                                                       \
  { const int inc_ = min(budget, EARLY_STEP); t0 += inc_; budget -= inc_; } \
  pace(t0 + (min(xt, PACE_REL * PACE_PER + PACE_OFF * 256) >> 8), lprev, slip_addr); \
  xt += 4 * PACE_PER;                                                  \
  _Pragma("unroll") for (int u_ = 4 * (g); u_ < 4 * (g) + 4; ++u_) { R[u_] = LDU(rn, u_); SB(); }
    const char *kp = (const char*)p.k0 + reg_off, *vp = (const char*)p.v0 + reg_off;
    int g = 2;
#pragma unroll 1
    for (int grp = 0;; ++grp) {
#pragma unroll 1
      do {
        const auto rn = __builtin_amdgcn_make_buffer_rsrc((void*)(g < 7 ? kp + (g + 1) * 65536 : vp), 0, 65536, RSRC_W3);
        float y[16];
#pragma unroll
        for (int i = 0; i < 16; ++i) {
          QK_STEP(R, i)
          if ((i & 3) == 3) { BURST(i >> 2) }
        }
        SCORES(g)
        SB();
      } while (++g < 8);
      m = max32(row_max(m));
      kp = (const char*)p.k1 + reg_off;
      budget += GAP_T;
      g = 0;
#pragma unroll 1
      do {
        const auto rn = __builtin_amdgcn_make_buffer_rsrc((void*)(g < 7 ? vp + (g + 1) * 65536 : kp), 0, 65536, RSRC_W3);
        PROBS(g)
        PV_STEP(0) PV_STEP(1) BURST(0) PV_STEP(2) PV_STEP(3) BURST(1) PV_STEP(4) PV_STEP(5) BURST(2) PV_STEP(6) PV_STEP(7) BURST(3)
      } while (++g < 8 - grp);
      if (grp) break;
      DUMP(0)
#pragma unroll
      for (int k = 0; k < 8; ++k) acc[0][k] = acc[1][k] = 0.f;
      m = -INFINITY;
      l = 0.f;
      q0 = *(const LAS v4u*)(L + L_Q + 1024 + (lane & 31) * 16);
      q1 = *(const LAS v4u*)(L + L_Q + 1536 + (lane & 31) * 16);
      vp = (const char*)p.v1 + reg_off;
      budget += GAP_T;
      g = 0;
    }
  }
  const unsigned t_end = t0 + __builtin_amdgcn_readfirstlane(*(volatile LAS unsigned*)(L + L_SLIP)) + ((512 * PACE_PER + PACE_OFF * 256) >> 8);
  {
    PROBS(7)
#define WO_AFTER(t) if (WO_A1 == (t)) { WO_LOADS(WO_E, (WO_E + 9) / 2) } if (WO_A2 == (t)) { WO_LOADS((WO_E + 9) / 2, 8) }
    PV_STEP(0) PV_STEP(1) PV_STEP(2) PV_STEP(3) WO_AFTER(3) PV_STEP(4) WO_AFTER(4) PV_STEP(5) WO_AFTER(5) PV_STEP(6) WO_AFTER(6) PV_STEP(7) WO_AFTER(7)
  }

  DUMP(1)
  WO_AFTER(8)
  LDS_BARRIER();
  WO_AFTER(9)
  {
    f32x2 ml[4];
    f32x4 ov[4];
#pragma unroll
    for (int i = 0; i < 4; ++i) {
      ml[i] = *(const LAS f32x2*)(L + L_ML + (w >> 1) * 64 + i * 16 + (w & 1) * 8);
      ov[i] = *(const LAS f32x4*)(L + L_MRG + (w >> 1) * 8192 + i * 2048 + (w & 1) * 1024 + voff);
    }
    const float M = fmaxf(fmaxf(ml[0].x, ml[1].x), fmaxf(ml[2].x, ml[3].x));
    f32x4 o = {0.f, 0.f, 0.f, 0.f};
    float ls = 0.f;
#pragma unroll
    for (int i = 0; i < 4; ++i) {
      const float e = __builtin_amdgcn_exp2f((ml[i].x - M) * SCL);
      o += e * ov[i];
      ls = fmaf(e, ml[i].y, ls);
    }
    const int og = WS_OG + ((w >> 1) * 256 + bid) * OG_STR;
    __builtin_amdgcn_raw_buffer_store_b128(enc4(o, tag), rws, voff, og + (w & 1) * 1024, AUX_SC1);
    __builtin_amdgcn_raw_buffer_store_b128(v4u{enc_f(M, tag), (__float_as_uint(M) & 0xffu) | tag << 8, enc_f(ls, tag), tag}, rws, lane == 0 ? 0 : OOB, og + 2048 + (w & 1) * 16, AUX_SC1);
  }
  WO_AFTER(10)
  wait_until(t_end + T_GATHER);

  {
    v4u g[4], gm;
    const int og_off = WS_OG + (br * 256 + kr * 32 + w * 8) * OG_STR, og_v = (lane >> 5) * OG_STR + (lane & 31) * 16;
    const int ml_v = (((lane & 3) * 2 + (lane >> 5)) ^ ((lane >> 2) & 1)) * OG_STR;
#define G_BAD() ((((gm.x ^ (tag << 24)) | (gm.z ^ (tag << 24)) | diff4(g[0], tag) | diff4(g[1], tag) | diff4(g[2], tag) | diff4(g[3], tag)) >> 24) | \
                 ((gm.y ^ (tag << 8)) >> 8) | (gm.w ^ tag))
#define GATHER()                                                                                          \
  gm = __builtin_amdgcn_raw_buffer_load_b128(rws, ml_v, og_off + 2048 + (rb >> 1) * 16, AUX_SC1);         \
  _Pragma("unroll") for (int s = 0; s < 4; ++s) g[s] = __builtin_amdgcn_raw_buffer_load_b128(rws, og_v, og_off + rb * 512 + s * 2 * OG_STR, AUX_SC1);
    GATHER()
    SB();
#pragma unroll
    for (int n = 0; n < 8; ++n) *(LAS v4u*)(L + L_WO + (32 * w + 4 * n + (lane >> 4)) * WO_STR + (lane & 15) * 16) = WO[n];
    SB();
    if (__any(G_BAD())) {
#pragma unroll 1
      for (unsigned spins = 0; spins < SPIN_CAP; ++spins) {
        MEMFENCE();
        GATHER()
        if (!__any(G_BAD())) break;
        __builtin_amdgcn_s_sleep(1);
      }
    }
    const float Ms = __uint_as_float(gm.x << 8 | (gm.y & 0xffu)), Ls = dec_f(gm.z);
    float Mx = fmaxf(Ms, dppf<XOR1>(Ms));
    Mx = fmaxf(Mx, dppf<XOR2>(Mx));
    Mx = fmaxf(Mx, dppf<XOR7>(Mx));
    const float e = __builtin_amdgcn_exp2f((Ms - Mx) * SCL);
    float Lt = e * Ls;
    Lt += dppf<XOR1>(Lt);
    Lt += dppf<XOR2>(Lt);
    Lt += dppf<XOR7>(Lt);
    const float c = e * __builtin_amdgcn_rcpf(Lt);
    float o0 = 0.f, o1 = 0.f, o2 = 0.f, o3 = 0.f;
#define MRG(s) { const float cs = dppf<BCAST + s>(c); o0 = fmaf(cs, dec_f(g[s].x), o0); o1 = fmaf(cs, dec_f(g[s].y), o1); o2 = fmaf(cs, dec_f(g[s].z), o2); o3 = fmaf(cs, dec_f(g[s].w), o3); }
    MRG(0) MRG(1) MRG(2) MRG(3)
    *(LAS unsigned*)(L + L_O + w * 256 + (lane & 31) * 8 + (lane >> 5) * 4) = cvtpk(sum32(o0, o2), sum32(o1, o3));
  }
  LDS_BARRIER();
  {
    v4u BW[2][4];
#pragma unroll
    for (int ks = 0; ks < 4; ++ks)
#pragma unroll
      for (int c = 0; c < 2; ++c) {
        LAS unsigned char* a = L + L_WO + (32 * ks + 8 * (lane >> 4) + ((lane >> 2) & 3)) * WO_STR + (2 * w + c) * 32 + (lane & 3) * 8;
        const v2u r0 = __builtin_bit_cast(v2u, __builtin_amdgcn_ds_read_tr16_b64_v4i16((LAS v4s*)a));
        const v2u r1 = __builtin_bit_cast(v2u, __builtin_amdgcn_ds_read_tr16_b64_v4i16((LAS v4s*)(a + 4 * WO_STR)));
        BW[c][ks] = v4u{r0.x, r0.y, r1.x, r1.y};
      }
    f32x4 d0 = {0.f, 0.f, 0.f, 0.f}, d1 = {0.f, 0.f, 0.f, 0.f};
#pragma unroll
    for (int ks = 0; ks < 4; ++ks) {
      const v4u o = *(const LAS v4u*)(L + L_O + (lane & 3) * 256 + ks * 64 + (lane >> 4) * 16);
      d0 = __builtin_amdgcn_mfma_f32_16x16x32_bf16(__builtin_bit_cast(bf16x8, BW[0][ks]), __builtin_bit_cast(bf16x8, o), d0, 0, 0, 0);
      d1 = __builtin_amdgcn_mfma_f32_16x16x32_bf16(__builtin_bit_cast(bf16x8, BW[1][ks]), __builtin_bit_cast(bf16x8, o), d1, 0, 0, 0);
    }
    const f32x4 d = b2 ? d1 : d0;
    __builtin_amdgcn_raw_buffer_store_b128(enc4(d, tag), rws,
                                           b3 ? OOB : (lane & 3) * 4096 + (lane & 4) * 16 + (lane >> 4) * 16,
                                           WS_PG + ((br * 4 + cb) * 4 + w) * 16384 + (kr * 4 + rb) * 128, AUX_SC1);
  }

  if (w == 3 && (bid & 1) == 0) {
    const int ri = bid >> 1;
    v4u g[4];
    DRAIN();
    wait_until(t_end + T_REDUCE);
#pragma unroll 1
    for (unsigned spins = 0; spins < SPIN_CAP; ++spins) {
      MEMFENCE();
#pragma unroll
      for (int s = 0; s < 4; ++s) g[s] = __builtin_amdgcn_raw_buffer_load_b128(rws, voff + s * 1024, WS_PG + ri * 4096, AUX_SC1);
      if (!__any((diff4(g[0], tag) | diff4(g[1], tag) | diff4(g[2], tag) | diff4(g[3], tag)) >> 24)) break;
      __builtin_amdgcn_s_sleep(1);
    }
    float sum[4];
#pragma unroll
    for (int k = 0; k < 4; ++k) {
      sum[k] = (dec_f(g[0][k]) + dec_f(g[1][k])) + (dec_f(g[2][k]) + dec_f(g[3][k]));
      sum[k] += dppf<ROR8>(sum[k]);
      sum[k] = sum16(sum[k], sum[k]);
      sum[k] = sum32(sum[k], sum[k]);
    }
    const auto ro = __builtin_amdgcn_make_buffer_rsrc(p.out, 0, 4 * 2 * DM * 2, RSRC_W3);
    __builtin_amdgcn_raw_buffer_store_b64(v2u{cvtpk(sum[0], sum[1]), cvtpk(sum[2], sum[3])}, ro, lane < ((ri & 12) == 12 ? 4 : 8) ? lane * 8 : OOB,
                                          ((ri & 3) * 2 * DM + (ri >> 6) * DM + ((ri >> 4) & 3) * 112 + ((ri >> 2) & 3) * 32) * 2, AUX_SC1);
    __builtin_amdgcn_raw_buffer_store_b32(tag, rws, (bid == 0 && lane == 0) ? 0 : OOB, WS_EPOCH, AUX_SC1);
  }
  asm volatile(".Ldec_code_end:");
}

}

extern "C" void kernel_launch(void* const* d_in, const int* in_sizes, int n_in, void* d_out, int out_size, void* d_ws, size_t ws_size,
                              hipStream_t stream) {
  Params p;
  p.x0 = d_in[0]; p.k0 = d_in[1]; p.v0 = d_in[2]; p.wq0 = d_in[3]; p.wo0 = d_in[4];
  p.x1 = d_in[5]; p.k1 = d_in[6]; p.v1 = d_in[7]; p.wq1 = d_in[8]; p.wo1 = d_in[9];
  p.out = d_out;
  p.ws = (unsigned char*)d_ws;
  decode_fused<<<256, 256, 0, stream>>>(p);
}
```

```cpp
#include <hip/hip_runtime.h>
#include <hip/hip_bf16.h>
#include <stdint.h>

namespace {

typedef unsigned v4u __attribute__((ext_vector_type(4)));
typedef unsigned v2u __attribute__((ext_vector_type(2)));
typedef unsigned long long v8p __attribute__((ext_vector_type(8)));
typedef unsigned long long v4p __attribute__((ext_vector_type(4)));
typedef float f32x4 __attribute__((ext_vector_type(4)));
typedef float f32x2 __attribute__((ext_vector_type(2)));
typedef short v4s __attribute__((ext_vector_type(4)));
typedef __bf16 bf16x2 __attribute__((ext_vector_type(2)));
typedef __bf16 bf16x8 __attribute__((ext_vector_type(8)));
#define LAS __attribute__((address_space(3)))
#define SB() __builtin_amdgcn_sched_barrier(0)
#define MEMFENCE() asm volatile("" ::: "memory")
#define KEEP(x) asm volatile("" :: "v"(x))
#define DRAIN() asm volatile("s_waitcnt vmcnt(0)" ::: "memory")
#define LDS_BARRIER() asm volatile("s_waitcnt lgkmcnt(0)\n\ts_barrier" ::: "memory")
#define NOW() ((unsigned)__builtin_amdgcn_s_memrealtime())

constexpr int DM = 448, NQ = 4096, SK = 8192, DH = 256;
constexpr unsigned REGION_BYTES = SK * DH * 2;

constexpr int PACE_GBS = 7820;
constexpr int PACE_OFF = 145;
constexpr int PACE_REL = 510;
constexpr int PUB_NOM = 204;
constexpr int EARLY = 44;
constexpr int EARLY_STEP = 4;
constexpr int SK_0 = 45;
constexpr int SK_K = 2;
constexpr int GAP_T = 0;
constexpr int SLIP_THR = 90;
constexpr int SLIP_N = 5;
constexpr int H_A = 8;
constexpr int H_P = 24;
constexpr int H_C = 32;
constexpr int H_S = 11;
constexpr int CKS = 4;
constexpr int TP = 3;
constexpr int CPF = 1;
constexpr int FT = 1;
constexpr int EPP = 1;
constexpr int WO_A1 = 7;
constexpr int WO_A2 = 9;
constexpr int WO_E = 2;
constexpr int WO_T = (WO_E * 13);
constexpr int T_GATHER = 150;
constexpr int T_REDUCE = 340;
constexpr int PACE_PER = (int)(1048576.0 * 256.0 / (PACE_GBS * 10.0) + 0.5);
static_assert(H_C >= 32 && H_C <= 44 && H_P <= H_C && H_A % 4 == 0 && H_P % 4 == 0 && H_C % 4 == 0, "head schedule");

constexpr int AUX_NT = 2, AUX_SC1 = 16, RSRC_W3 = 0x00020000;
constexpr int OOB = 0x7fff0000;
constexpr unsigned SPIN_CAP = 1u << 18;
constexpr float SCL = 0.0625f * 1.4426950408889634f;

constexpr unsigned WS_EPOCH = 0;
constexpr unsigned WS_QG = 32768;
constexpr unsigned OG_STR = 2048 + 128;
constexpr unsigned WS_OG = WS_QG + 256 * 512;
constexpr unsigned WS_PG = WS_OG + 2 * 256 * OG_STR;
constexpr unsigned WS_END = WS_PG + 128 * 4096;

constexpr int WO_STR = 288;
constexpr int L_WO = 0;
constexpr int L_MRG = 128 * WO_STR;
constexpr int L_QP = L_MRG + 16384;
constexpr int L_Q = L_QP + 2048;
constexpr int L_O = L_Q + 2048;
constexpr int L_ML = L_O + 1024;
constexpr int L_SLIP = L_ML + 128;
constexpr int L_SC = L_SLIP + 16;
constexpr int L_BYTES = 84 * 1024;
static_assert(L_SC + 8 * 1024 <= L_BYTES && L_SC % 16 == 0, "lds map");

struct Params {
  const void *x0, *k0, *v0, *wq0, *wo0, *x1, *k1, *v1, *wq1, *wo1;
  void* out;
  unsigned char* ws;
};

__device__ __forceinline__ float dot2(unsigned a, unsigned b, float c) {
  return __builtin_amdgcn_fdot2_f32_bf16(__builtin_bit_cast(bf16x2, a), __builtin_bit_cast(bf16x2, b), c, false);
}
__device__ __forceinline__ unsigned pk_lo(unsigned a, unsigned b) { return __builtin_amdgcn_perm(b, a, 0x05040100u); }
__device__ __forceinline__ unsigned pk_hi(unsigned a, unsigned b) { return __builtin_amdgcn_perm(b, a, 0x07060302u); }
__device__ __forceinline__ unsigned cvtpk(float lo, float hi) {
  bf16x2 c = {(__bf16)lo, (__bf16)hi};
  return __builtin_bit_cast(unsigned, c);
}
template <int CTRL> __device__ __forceinline__ float dppf(float x) {
  return __builtin_bit_cast(float, __builtin_amdgcn_mov_dpp(__builtin_bit_cast(int, x), CTRL, 0xf, 0xf, true));
}
template <int CTRL> __device__ __forceinline__ unsigned dppu(unsigned x) {
  return (unsigned)__builtin_amdgcn_mov_dpp((int)x, CTRL, 0xf, 0xf, true);
}
constexpr int XOR1 = 0xB1, XOR2 = 0x4E, XOR7 = 0x141, ROR8 = 0x128, ROR4 = 0x124, ROR2 = 0x122, ROR1 = 0x121, BCAST = 0x150;
__device__ __forceinline__ float row_sum(float a) {
  a += dppf<ROR8>(a); a += dppf<ROR4>(a); a += dppf<ROR2>(a);
  return a + dppf<ROR1>(a);
}
__device__ __forceinline__ float row_max(float a) {
  a = fmaxf(a, dppf<ROR8>(a)); a = fmaxf(a, dppf<ROR4>(a)); a = fmaxf(a, dppf<ROR2>(a));
  return fmaxf(a, dppf<ROR1>(a));
}
__device__ __forceinline__ float sum16(float a, float b) {
  auto r = __builtin_amdgcn_permlane16_swap(__float_as_uint(a), __float_as_uint(b), false, false);
  return __uint_as_float(r[0]) + __uint_as_float(r[1]);
}
__device__ __forceinline__ float sum32(float a, float b) {
  auto r = __builtin_amdgcn_permlane32_swap(__float_as_uint(a), __float_as_uint(b), false, false);
  return __uint_as_float(r[0]) + __uint_as_float(r[1]);
}
__device__ __forceinline__ float max32(float a) {
  auto r = __builtin_amdgcn_permlane32_swap(__float_as_uint(a), __float_as_uint(a), false, false);
  return fmaxf(__uint_as_float(r[0]), __uint_as_float(r[1]));
}
constexpr unsigned TAG_INC = 0x35;
__device__ __forceinline__ unsigned enc_f(float f, unsigned tag) { return __builtin_amdgcn_alignbit(tag, __float_as_uint(f), 8); }
__device__ __forceinline__ v4u enc4(f32x4 f, unsigned tag) {
  return v4u{enc_f(f.x, tag), enc_f(f.y, tag + TAG_INC), enc_f(f.z, tag + 2 * TAG_INC), enc_f(f.w, tag + 3 * TAG_INC)};
}
__device__ __forceinline__ float dec_f(unsigned d) { return __uint_as_float(d << 8); }
__device__ __forceinline__ unsigned diff4(v4u g, unsigned tag) {
  return ((g.x ^ (tag << 24)) | (g.y ^ ((tag + TAG_INC) << 24))) | ((g.z ^ ((tag + 2 * TAG_INC) << 24)) | (g.w ^ ((tag + 3 * TAG_INC) << 24)));
}

__device__ __forceinline__ void wait_until(unsigned t) {
  SB();
  asm volatile("1:\n\ts_memrealtime vcc\n\ts_waitcnt lgkmcnt(0)\n\ts_sub_u32 vcc_lo, vcc_lo, %0\n\ts_cmp_lt_i32 vcc_lo, 0\n\t"
               "s_cbranch_scc0 2f\n\ts_sleep 1\n\ts_branch 1b\n2:" :: "s"(__builtin_amdgcn_readfirstlane(t)) : "vcc", "scc", "memory");
  SB();
}
__device__ __forceinline__ void pace(unsigned t, int& lprev, unsigned slip_addr) {
  unsigned vt, slip, tgt;
  int lp = __builtin_amdgcn_readfirstlane(lprev);
  SB();
  asm volatile(
      "ds_read_b32 %0, %5\n\ts_memrealtime vcc\n\ts_waitcnt lgkmcnt(0)\n\t"
      "v_readfirstlane_b32 %1, %0\n\t"
      "s_add_u32 %2, %1, %4\n\t"
      "s_sub_u32 vcc_lo, vcc_lo, %2\n\t"
      "s_lshl_b32 %3, %3, 4\n\ts_sub_i32 vcc_hi, vcc_lo, %3\n\t"
      "s_cmp_gt_i32 vcc_hi, %7\n\t"
      "s_cbranch_scc0 3f\n\t"
      "s_add_u32 %1, %1, %6\n\ts_add_u32 %2, %2, %6\n\ts_sub_u32 vcc_lo, vcc_lo, %6\n\t"
      "v_mov_b32 %0, %1\n\tds_max_u32 %5, %0\n"
      "3:\n\ts_max_i32 %3, vcc_lo, 0\n"
      "1:\n\ts_cmp_lt_i32 vcc_lo, 0\n\ts_cbranch_scc0 2f\n\ts_sleep 1\n\t"
      "s_memrealtime vcc\n\ts_waitcnt lgkmcnt(0)\n\ts_sub_u32 vcc_lo, vcc_lo, %2\n\ts_branch 1b\n"
      "2:\n\ts_waitcnt lgkmcnt(0)"
      : "=&v"(vt), "=&s"(slip), "=&s"(tgt), "+s"(lp)
      : "s"(__builtin_amdgcn_readfirstlane(t)), "v"(slip_addr), "i"((SLIP_N * 4 * PACE_PER + 128) >> 8), "i"(SLIP_THR)
      : "vcc", "scc", "memory");
  lprev = __builtin_amdgcn_readfirstlane(lp);
  SB();
}


__global__ void __launch_bounds__(256, 1) decode_fused(Params args) {
  __shared__ __attribute__((aligned(16))) unsigned char smem[L_BYTES];
  LAS unsigned char* L = (LAS unsigned char*)smem;
  v8p arg_a;
  v4p arg_b;
  unsigned long long clk0;
  asm volatile("s_load_dwordx16 %0, %3, 0x0\n\ts_load_dwordx8 %1, %3, 0x40\n\ts_memrealtime %2"
               : "=&s"(arg_a), "=&s"(arg_b), "=&s"(clk0) : "s"(__builtin_amdgcn_kernarg_segment_ptr()));
  SB();
  const int bid = blockIdx.x, kvh = bid >> 5, b = (bid >> 3) & 3, sp = bid & 7;
  const int br = bid >> 7, j = (bid >> 3) & 15, kr = (sp + 4 * br) & 7, cb = j & 3, rb = j >> 2;
  const int tid = threadIdx.x, lane = tid & 63, w = __builtin_amdgcn_readfirstlane(tid >> 6);
  const int voff = lane * 16, koff = tid * 16;
  const bool b3 = lane & 8, b2 = lane & 4, b1 = lane & 2, b0 = lane & 1;
  LAS float* const sc = (LAS float*)(L + L_SC + tid * 4);

  unsigned cpf = 0;
  unsigned long long pc;
  unsigned clen;
  asm volatile("s_getpc_b64 %0\n.Ldec_code_begin:\n\ts_mov_b32 %1, .Ldec_code_end-.Ldec_code_begin" : "=s"(pc), "=s"(clen));
#define CODE_PF(n) if (CPF == (n)) cpf = __builtin_amdgcn_raw_buffer_load_b32(__builtin_amdgcn_make_buffer_rsrc((void*)pc, 0, clen, RSRC_W3), tid < 8 ? tid * 128 : OOB, (bid >> 3) * 1024, 0);
  CODE_PF(1)
  const int row0 = 128 * w + 8 * (lane >> 2);
  int xoff = row0 < DM ? row0 * 2 : OOB, woff = row0 * (NQ * 2) + (lane & 3) * 16, wso = (kr * 512 + j * 32) * 2;
  int poffv = w == 3 && lane < 56 ? (lane >> 1) * (16 * NQ * 2) + (lane & 1) * 64 + kr * 1024 + j * (NQ * 2) : OOB;
  int eso = w == 0 ? WS_EPOCH : OOB;
  asm volatile("" : "+v"(xoff), "+v"(woff), "+v"(poffv));
  SB();
  asm volatile("s_waitcnt lgkmcnt(0)" : "+s"(arg_a), "+s"(arg_b), "+s"(clk0), "+s"(wso), "+s"(eso));
  SB();
  Params p;
  p.x0 = (void*)arg_a[0]; p.k0 = (void*)arg_a[1]; p.v0 = (void*)arg_a[2]; p.wq0 = (void*)arg_a[3]; p.wo0 = (void*)arg_a[4];
  p.x1 = (void*)arg_a[5]; p.k1 = (void*)arg_a[6]; p.v1 = (void*)arg_a[7]; p.wq1 = (void*)arg_b[0]; p.wo1 = (void*)arg_b[1];
  p.out = (void*)arg_b[2]; p.ws = (unsigned char*)arg_b[3];
  unsigned t0 = (unsigned)clk0;
  const auto rws = __builtin_amdgcn_make_buffer_rsrc(p.ws, 0, WS_END, RSRC_W3);

  v4u X[4], W[8], X0[16], X1[16], R[16];
  unsigned pfw, ft = 0;
  unsigned ep;
  {
    const auto rx = __builtin_amdgcn_make_buffer_rsrc((void*)(br ? p.x1 : p.x0), 0, 4 * DM * 2, RSRC_W3);
    const auto rwq = __builtin_amdgcn_make_buffer_rsrc((void*)(br ? p.wq1 : p.wq0), 0, DM * NQ * 2, RSRC_W3);
    if (FT) {
      const size_t wqp = (size_t)(br ? p.wq1 : p.wq0) + wso;
      const size_t a = (j & 2) ? ((j & 1) ? (size_t)p.ws + WS_QG + bid * 512 : wqp + 447 * (NQ * 2)) : wqp;
      ft = __builtin_amdgcn_raw_buffer_load_b32(__builtin_amdgcn_make_buffer_rsrc((void*)a, 0, (j & 3) ? 4 : 0, RSRC_W3), 0, 0, AUX_NT);
      SB();
    }
#pragma unroll
    for (int i = 0; i < 4; ++i) X[i] = __builtin_amdgcn_raw_buffer_load_b128(rx, xoff + i * DM * 2, 0, 0);
    if (EPP == 0) ep = __builtin_amdgcn_raw_buffer_load_b32(rws, 0, eso, AUX_SC1);
    pfw = __builtin_amdgcn_raw_buffer_load_b32(rwq, poffv, (384u - (unsigned)(size_t)(br ? p.wq1 : p.wq0)) & 896u, AUX_NT);
    SB();
#pragma unroll
    for (int n = 0; n < 8; ++n) W[n] = __builtin_amdgcn_raw_buffer_load_b128(rwq, woff, wso + n * (NQ * 2), AUX_NT);
    SB();
    if (EPP == 1) ep = __builtin_amdgcn_raw_buffer_load_b32(rws, 0, eso, AUX_SC1);
    CODE_PF(2)
    SB();
  }
  asm volatile("" : "+s"(p.wo0), "+s"(p.wo1), "+s"(p.v1));
  const size_t reg_off = (size_t)(b * 8 + kvh) * REGION_BYTES + sp * (REGION_BYTES / 8);
  const char* const wop = (const char*)(br ? p.wo1 : p.wo0) + (kr * 512 + rb * 128) * (DM * 2) + cb * 224;
  unsigned tch = 0;
  const size_t ta12 = w == 1 ? (size_t)p.v0 : (size_t)p.k1, ta03 = w == 3 ? (size_t)p.v1 : (size_t)wop;
  const size_t tadr = (w == 1 || w == 2 ? ta12 : ta03) + (w ? reg_off : 0);
#define TOUCH_IF(c) { tch |= __builtin_amdgcn_raw_buffer_load_b32(__builtin_amdgcn_make_buffer_rsrc((void*)tadr, 0, (c) ? 65536 : 0, RSRC_W3), lane == 0 ? 0 : OOB, ((bid * 3 + w) & 63) * 896, AUX_NT); SB(); }
#define TOUCH_AT(n) if (TP == (n)) { tch = __builtin_amdgcn_raw_buffer_load_b32(__builtin_amdgcn_make_buffer_rsrc((void*)tadr, 0, 65536, RSRC_W3), lane == 0 ? 0 : OOB, ((bid * 3 + w) & 63) * 896, AUX_NT); SB(); }
  TOUCH_AT(0)
#define LDU(rs, i) __builtin_amdgcn_raw_buffer_load_b128(rs, koff, (i) * 4096, AUX_NT)
  const int poff = PACE_OFF * 256 + (((b * 8 + kvh) * 19) & 31) * (4 * PACE_PER / 32);
  const auto rk0 = __builtin_amdgcn_make_buffer_rsrc((char*)p.k0 + reg_off, 0, 48 * 4096, RSRC_W3);
#define ISSUE(lo, hi)                                                                     \
  _Pragma("unroll") for (int i_ = (lo); i_ < (hi) && i_ < 48; ++i_) {                     \
    if (i_ >= H_A + 4 && (i_ & 3) == 0) wait_until(t0 + ((i_ * PACE_PER + poff) >> 8));   \
    if (i_ < 16) X0[i_ & 15] = LDU(rk0, i_);                                              \
    else if (i_ < 32) X1[i_ & 15] = LDU(rk0, i_);                                         \
    else R[i_ & 15] = LDU(rk0, i_);                                                       \
    SB();                                                                                 \
  }
  ISSUE(0, H_A)
  TOUCH_AT(1)

  {
    float a[4][8];
#pragma unroll
    for (int bb = 0; bb < 4; ++bb)
#pragma unroll
      for (int c = 0; c < 8; ++c) a[bb][c] = 0.f;
#pragma unroll
    for (int t = 0; t < 4; ++t) {
#pragma unroll
      for (int k = 0; k < 4; ++k) {
        const unsigned lo = pk_lo(W[2 * t][k], W[2 * t + 1][k]), hi = pk_hi(W[2 * t][k], W[2 * t + 1][k]);
#pragma unroll
        for (int bb = 0; bb < 4; ++bb) {
          a[bb][2 * k] = dot2(lo, X[bb][t], a[bb][2 * k]);
          a[bb][2 * k + 1] = dot2(hi, X[bb][t], a[bb][2 * k + 1]);
        }
      }
      SB();
      ISSUE(H_A + t, H_A + t + 1)
    }
    float y[8];
#pragma unroll
    for (int c = 0; c < 8; ++c) {
      y[c] = sum16(sum32(a[0][c], a[2][c]), sum32(a[1][c], a[3][c]));
      y[c] += dppf<ROR8>(y[c]);
      y[c] += dppf<ROR4>(y[c]);
    }
    LAS f32x4* qp = (LAS f32x4*)(L + L_QP + w * 512 + (lane >> 4) * 128 + (lane & 3) * 32);
    qp[0] = f32x4{y[0], y[1], y[2], y[3]};
    qp[1] = f32x4{y[4], y[5], y[6], y[7]};
  }
  *(LAS unsigned*)(L + (w == 0 ? L_ML : L_O)) = ep;
  LDS_BARRIER();
  const unsigned tag = __builtin_amdgcn_readfirstlane(*(volatile LAS unsigned*)(L + L_ML)) + 1u;
  if (w == 0) {
    const unsigned tt = ((NOW() >> 2) & 0xffu) | tag << 8;
    const LAS f32x4* qp = (const LAS f32x4*)(L + L_QP + (lane & 31) * 16);
    const f32x4 q = (qp[0] + qp[32]) + (qp[64] + qp[96]);
    const unsigned lo = cvtpk(q.x, q.y), hi = cvtpk(q.z, q.w);
    __builtin_amdgcn_raw_buffer_store_b128(v4u{pk_lo(lo, tt), __builtin_amdgcn_alignbit(tt + (TAG_INC << 8), lo, 16), pk_lo(hi, tt + (2 * TAG_INC << 8)),
                                               __builtin_amdgcn_alignbit(tt + (3 * TAG_INC << 8), hi, 16)},
                                           rws, lane < 32 ? voff : OOB, WS_QG + (kr * 32 + br * 16 + j) * 512, AUX_SC1);
  }
  SB();
  TOUCH_AT(2)
  ISSUE(H_A + 4, H_A + 8)
  TOUCH_AT(3)
  ISSUE(H_A + 8, H_P)
  int budget;
  {
    const int qg_off = WS_QG + kvh * 32 * 512 + b * 128, qg_v = (tid >> 3) * 512 + (tid & 7) * 16;
    const int ck_v = w == 0 ? (lane & 15) * (16 * 512) + CKS * 512 : OOB;
    const unsigned ck_mask = w == 0 ? ~0u : 0u;
    v4u qg = __builtin_amdgcn_raw_buffer_load_b128(rws, qg_v, qg_off, AUX_SC1);
    v4u ck = __builtin_amdgcn_raw_buffer_load_b128(rws, ck_v, WS_QG, AUX_SC1);
    SB();
    ISSUE(H_P, H_C)
    TOUCH_AT(4)
    KEEP(cpf);
    KEEP(ft);
    KEEP(pfw);
    if (__any((diff4(qg, tag) | (diff4(ck, tag) & ck_mask)) >> 24)) {
#pragma unroll 1
      for (unsigned spins = 0; spins < SPIN_CAP; ++spins) {
        MEMFENCE();
        qg = __builtin_amdgcn_raw_buffer_load_b128(rws, qg_v, qg_off, AUX_SC1);
        ck = __builtin_amdgcn_raw_buffer_load_b128(rws, ck_v, WS_QG, AUX_SC1);
        if (!__any((diff4(qg, tag) | (diff4(ck, tag) & ck_mask)) >> 24)) break;
        __builtin_amdgcn_s_sleep(2);
      }
    }
    *(LAS v2u*)(L + L_Q + tid * 8) = v2u{pk_lo(qg.x, qg.y), pk_lo(qg.z, qg.w)};
    const unsigned ref = (t0 + PUB_NOM) & ~3u;
    const float d = (float)(int)(signed char)((ck.x >> 16) - (ref >> 2));
    const int mean = (int)row_sum(d) >> 2, skew = (int)row_max(d) * 4 - mean;
    *(LAS v4u*)(L + (w == 0 ? L_SLIP : L_O)) = v4u{0u, ref + (unsigned)mean - PUB_NOM, (unsigned)skew, 0u};
  }
  LDS_BARRIER();
  {
    const v4u an = *(const LAS v4u*)(L + L_SLIP);
    t0 = __builtin_amdgcn_readfirstlane(t0 + min(max((int)(an.y - t0), -150), 400));
    budget = __builtin_amdgcn_readfirstlane(EARLY + ((min(max((int)an.z - SK_0, 0), 200) * SK_K) >> 2));
  }
  v4u q0 = *(const LAS v4u*)(L + L_Q + (lane & 31) * 16), q1 = *(const LAS v4u*)(L + L_Q + 512 + (lane & 31) * 16);

  float acc[2][8], m = -INFINITY, l = 0.f;
#pragma unroll
  for (int k = 0; k < 8; ++k) acc[0][k] = acc[1][k] = 0.f;
#define QK_STEP(KR, i)                                                 \
  {                                                                    \
    float s0 = dot2(KR[i][0], q0[0], 0.f), s1 = dot2(KR[i][0], q1[0], 0.f); \
    s0 = dot2(KR[i][1], q0[1], s0); s1 = dot2(KR[i][1], q1[1], s1);    \
    s0 = dot2(KR[i][2], q0[2], s0); s1 = dot2(KR[i][2], q1[2], s1);    \
    s0 = dot2(KR[i][3], q0[3], s0); s1 = dot2(KR[i][3], q1[3], s1);    \
    y[i] = sum16(s0, s1);                                              \
  }
#define TR_STEP(H, CTRL, BIT)                                          \
  _Pragma("unroll") for (int i = 0; i < (H); ++i) {                    \
    const float keep = (BIT) ? y[i + (H)] : y[i], send = (BIT) ? y[i] : y[i + (H)]; \
    y[i] = keep + dppf<CTRL>(send);                                    \
  }
#define SCORES(slot)                                                   \
  TR_STEP(8, ROR8, b3) TR_STEP(4, XOR7, b2) TR_STEP(2, XOR2, b1) TR_STEP(1, XOR1, b0) \
  m = fmaxf(m, y[0]);                                                  \
  sc[(slot) * 256] = y[0];
#define PROBS(slot)                                                                     \
  const float pp = __builtin_amdgcn_exp2f((sc[(slot) * 256] - m) * SCL);                \
  l += pp;                                                                              \
  const unsigned ppk = cvtpk(pp, dppf<XOR1>(pp));        \
  const auto ph = __builtin_amdgcn_permlane16_swap(ppk, ppk, false, false);
#define PV_STEP(t)                                                     \
  {                                                                    \
    const unsigned p0 = dppu<BCAST + 2 * (t)>(ph[0]), p1 = dppu<BCAST + 2 * (t)>(ph[1]); \
    _Pragma("unroll") for (int k = 0; k < 4; ++k) {                    \
      const unsigned lo = pk_lo(R[2 * (t)][k], R[2 * (t) + 1][k]), hi = pk_hi(R[2 * (t)][k], R[2 * (t) + 1][k]); \
      acc[0][2 * k] = dot2(lo, p0, acc[0][2 * k]); acc[0][2 * k + 1] = dot2(hi, p0, acc[0][2 * k + 1]); \
      acc[1][2 * k] = dot2(lo, p1, acc[1][2 * k]); acc[1][2 * k + 1] = dot2(hi, p1, acc[1][2 * k + 1]); \
    }                                                                  \
  }
#define DUMP(brn)                                                      \
  {                                                                    \
    float o[8];                                                        \
    _Pragma("unroll") for (int k = 0; k < 8; ++k) o[k] = sum32(acc[0][k], acc[1][k]);     \
    LAS f32x4* mp = (LAS f32x4*)(L + L_MRG + (brn) * 8192 + tid * 32); \
    mp[0] = f32x4{o[0], o[1], o[2], o[3]};                             \
    mp[1] = f32x4{o[4], o[5], o[6], o[7]};                             \
    l = row_sum(l);                                                    \
    l = sum32(l, l);                                                   \
    *(LAS f32x2*)(L + L_ML + (brn) * 64 + w * 16 + ((lane >> 4) & 1) * 8) = f32x2{m, l};       \
  }
  {
    float y[16];
#pragma unroll
    for (int i = 0; i < 16; ++i) {
      if (i % H_S == 0) { SB(); ISSUE(H_C + (i / H_S) * 4, H_C + (i / H_S) * 4 + 4) }
      QK_STEP(X0, i)
    }
    SCORES(0)
#pragma unroll
    for (int i = 0; i < 16; ++i) {
      if ((i + 16) % H_S == 0) { SB(); ISSUE(H_C + ((i + 16) / H_S) * 4, H_C + ((i + 16) / H_S) * 4 + 4) }
      QK_STEP(X1, i)
    }
    SCORES(1)
    SB();
  }
  ISSUE(H_C + (31 / H_S + 1) * 4, 48)
  v4u WO[8];
  const int ooff = (lane & 15) < 14 ? (lane >> 4) * (DM * 2) + (lane & 15) * 16 : OOB;
#define WO_LOADS(lo, hi)                                                                  \
  SB();                                                                                   \
  _Pragma("unroll") for (int n = (lo); n < (hi); ++n)                                     \
    WO[n] = __builtin_amdgcn_raw_buffer_load_b128(__builtin_amdgcn_make_buffer_rsrc((void*)wop, 0, 127 * DM * 2 + 224, RSRC_W3), ooff, \
                                                  (32 * w + 4 * n) * (DM * 2), AUX_NT);   \
  SB();
  wait_until(t0 + ((48 * PACE_PER + poff) >> 8));
  KEEP(tch);
  WO_LOADS(0, WO_E)
  t0 += WO_T;
  {
    const unsigned slip_addr = (unsigned)(unsigned long)(L + L_SLIP);
    int lprev = 1 << 20;
    int xt = 48 * PACE_PER + poff;
#define BURST(g)                                                       \
  { const int inc_ = min(budget, EARLY_STEP); t0 += inc_; budget -= inc_; } \
  pace(t0 + (min(xt, PACE_REL * PACE_PER + PACE_OFF * 256) >> 8), lprev, slip_addr); \
  xt += 4 * PACE_PER;                                                  \
  _Pragma("unroll") for (int u_ = 4 * (g); u_ < 4 * (g) + 4; ++u_) { R[u_] = LDU(rn, u_); SB(); }
    const char *kp = (const char*)p.k0 + reg_off, *vp = (const char*)p.v0 + reg_off;
    int g = 2;
#pragma unroll 1
    for (int grp = 0;; ++grp) {
#pragma unroll 1
      do {
        const auto rn = __builtin_amdgcn_make_buffer_rsrc((void*)(g < 7 ? kp + (g + 1) * 65536 : vp), 0, 65536, RSRC_W3);
        float y[16];
#pragma unroll
        for (int i = 0; i < 16; ++i) {
          QK_STEP(R, i)
          if ((i & 3) == 3) { BURST(i >> 2) }
        }
        SCORES(g)
        SB();
      } while (++g < 8);
      m = max32(row_max(m));
      kp = (const char*)p.k1 + reg_off;
      budget += GAP_T;
      g = 0;
#pragma unroll 1
      do {
        const auto rn = __builtin_amdgcn_make_buffer_rsrc((void*)(g < 7 ? vp + (g + 1) * 65536 : kp), 0, 65536, RSRC_W3);
        PROBS(g)
        PV_STEP(0) PV_STEP(1) BURST(0) PV_STEP(2) PV_STEP(3) BURST(1) PV_STEP(4) PV_STEP(5) BURST(2) PV_STEP(6) PV_STEP(7) BURST(3)
      } while (++g < 8 - grp);
      if (grp) break;
      DUMP(0)
#pragma unroll
      for (int k = 0; k < 8; ++k) acc[0][k] = acc[1][k] = 0.f;
      m = -INFINITY;
      l = 0.f;
      q0 = *(const LAS v4u*)(L + L_Q + 1024 + (lane & 31) * 16);
      q1 = *(const LAS v4u*)(L + L_Q + 1536 + (lane & 31) * 16);
      vp = (const char*)p.v1 + reg_off;
      budget += GAP_T;
      g = 0;
    }
  }
  const unsigned t_end = t0 + __builtin_amdgcn_readfirstlane(*(volatile LAS unsigned*)(L + L_SLIP)) + ((512 * PACE_PER + PACE_OFF * 256) >> 8);
  {
    PROBS(7)
#define WO_AFTER(t) if (WO_A1 == (t)) { WO_LOADS(WO_E, (WO_E + 9) / 2) } if (WO_A2 == (t)) { WO_LOADS((WO_E + 9) / 2, 8) }
    PV_STEP(0) PV_STEP(1) PV_STEP(2) PV_STEP(3) WO_AFTER(3) PV_STEP(4) WO_AFTER(4) PV_STEP(5) WO_AFTER(5) PV_STEP(6) WO_AFTER(6) PV_STEP(7) WO_AFTER(7)
  }

  DUMP(1)
  WO_AFTER(8)
  LDS_BARRIER();
  WO_AFTER(9)
  {
    f32x2 ml[4];
    f32x4 ov[4];
#pragma unroll
    for (int i = 0; i < 4; ++i) {
      ml[i] = *(const LAS f32x2*)(L + L_ML + (w >> 1) * 64 + i * 16 + (w & 1) * 8);
      ov[i] = *(const LAS f32x4*)(L + L_MRG + (w >> 1) * 8192 + i * 2048 + (w & 1) * 1024 + voff);
    }
    const float M = fmaxf(fmaxf(ml[0].x, ml[1].x), fmaxf(ml[2].x, ml[3].x));
    f32x4 o = {0.f, 0.f, 0.f, 0.f};
    float ls = 0.f;
#pragma unroll
    for (int i = 0; i < 4; ++i) {
      const float e = __builtin_amdgcn_exp2f((ml[i].x - M) * SCL);
      o += e * ov[i];
      ls = fmaf(e, ml[i].y, ls);
    }
    const int og = WS_OG + ((w >> 1) * 256 + bid) * OG_STR;
    __builtin_amdgcn_raw_buffer_store_b128(enc4(o, tag), rws, voff, og + (w & 1) * 1024, AUX_SC1);
    __builtin_amdgcn_raw_buffer_store_b128(v4u{enc_f(M, tag), (__float_as_uint(M) & 0xffu) | tag << 8, enc_f(ls, tag), tag}, rws, lane == 0 ? 0 : OOB, og + 2048 + (w & 1) * 16, AUX_SC1);
  }
  WO_AFTER(10)
  wait_until(t_end + T_GATHER);

  {
    v4u g[4], gm;
    const int og_off = WS_OG + (br * 256 + kr * 32 + w * 8) * OG_STR, og_v = (lane >> 5) * OG_STR + (lane & 31) * 16;
    const int ml_v = (((lane & 3) * 2 + (lane >> 5)) ^ ((lane >> 2) & 1)) * OG_STR;
#define G_BAD() ((((gm.x ^ (tag << 24)) | (gm.z ^ (tag << 24)) | diff4(g[0], tag) | diff4(g[1], tag) | diff4(g[2], tag) | diff4(g[3], tag)) >> 24) | \
                 ((gm.y ^ (tag << 8)) >> 8) | (gm.w ^ tag))
#define GATHER()                                                                                          \
  gm = __builtin_amdgcn_raw_buffer_load_b128(rws, ml_v, og_off + 2048 + (rb >> 1) * 16, AUX_SC1);         \
  _Pragma("unroll") for (int s = 0; s < 4; ++s) g[s] = __builtin_amdgcn_raw_buffer_load_b128(rws, og_v, og_off + rb * 512 + s * 2 * OG_STR, AUX_SC1);
    GATHER()
    SB();
#pragma unroll
    for (int n = 0; n < 8; ++n) *(LAS v4u*)(L + L_WO + (32 * w + 4 * n + (lane >> 4)) * WO_STR + (lane & 15) * 16) = WO[n];
    SB();
    if (__any(G_BAD())) {
#pragma unroll 1
      for (unsigned spins = 0; spins < SPIN_CAP; ++spins) {
        MEMFENCE();
        GATHER()
        if (!__any(G_BAD())) break;
        __builtin_amdgcn_s_sleep(1);
      }
    }
    const float Ms = __uint_as_float(gm.x << 8 | (gm.y & 0xffu)), Ls = dec_f(gm.z);
    float Mx = fmaxf(Ms, dppf<XOR1>(Ms));
    Mx = fmaxf(Mx, dppf<XOR2>(Mx));
    Mx = fmaxf(Mx, dppf<XOR7>(Mx));
    const float e = __builtin_amdgcn_exp2f((Ms - Mx) * SCL);
    float Lt = e * Ls;
    Lt += dppf<XOR1>(Lt);
    Lt += dppf<XOR2>(Lt);
    Lt += dppf<XOR7>(Lt);
    const float c = e * __builtin_amdgcn_rcpf(Lt);
    float o0 = 0.f, o1 = 0.f, o2 = 0.f, o3 = 0.f;
#define MRG(s) { const float cs = dppf<BCAST + s>(c); o0 = fmaf(cs, dec_f(g[s].x), o0); o1 = fmaf(cs, dec_f(g[s].y), o1); o2 = fmaf(cs, dec_f(g[s].z), o2); o3 = fmaf(cs, dec_f(g[s].w), o3); }
    MRG(0) MRG(1) MRG(2) MRG(3)
    *(LAS unsigned*)(L + L_O + w * 256 + (lane & 31) * 8 + (lane >> 5) * 4) = cvtpk(sum32(o0, o2), sum32(o1, o3));
  }
  LDS_BARRIER();
  {
    v4u BW[2][4];
#pragma unroll
    for (int ks = 0; ks < 4; ++ks)
#pragma unroll
      for (int c = 0; c < 2; ++c) {
        LAS unsigned char* a = L + L_WO + (32 * ks + 8 * (lane >> 4) + ((lane >> 2) & 3)) * WO_STR + (2 * w + c) * 32 + (lane & 3) * 8;
        const v2u r0 = __builtin_bit_cast(v2u, __builtin_amdgcn_ds_read_tr16_b64_v4i16((LAS v4s*)a));
        const v2u r1 = __builtin_bit_cast(v2u, __builtin_amdgcn_ds_read_tr16_b64_v4i16((LAS v4s*)(a + 4 * WO_STR)));
        BW[c][ks] = v4u{r0.x, r0.y, r1.x, r1.y};
      }
    f32x4 d0 = {0.f, 0.f, 0.f, 0.f}, d1 = {0.f, 0.f, 0.f, 0.f};
#pragma unroll
    for (int ks = 0; ks < 4; ++ks) {
      const v4u o = *(const LAS v4u*)(L + L_O + (lane & 3) * 256 + ks * 64 + (lane >> 4) * 16);
      d0 = __builtin_amdgcn_mfma_f32_16x16x32_bf16(__builtin_bit_cast(bf16x8, BW[0][ks]), __builtin_bit_cast(bf16x8, o), d0, 0, 0, 0);
      d1 = __builtin_amdgcn_mfma_f32_16x16x32_bf16(__builtin_bit_cast(bf16x8, BW[1][ks]), __builtin_bit_cast(bf16x8, o), d1, 0, 0, 0);
    }
    const f32x4 d = b2 ? d1 : d0;
    __builtin_amdgcn_raw_buffer_store_b128(enc4(d, tag), rws,
                                           b3 ? OOB : (lane & 3) * 4096 + (lane & 4) * 16 + (lane >> 4) * 16,
                                           WS_PG + ((br * 4 + cb) * 4 + w) * 16384 + (kr * 4 + rb) * 128, AUX_SC1);
  }

  if (w == 3 && (bid & 1) == 0) {
    const int ri = bid >> 1;
    v4u g[4];
    DRAIN();
    wait_until(t_end + T_REDUCE);
#pragma unroll 1
    for (unsigned spins = 0; spins < SPIN_CAP; ++spins) {
      MEMFENCE();
#pragma unroll
      for (int s = 0; s < 4; ++s) g[s] = __builtin_amdgcn_raw_buffer_load_b128(rws, voff + s * 1024, WS_PG + ri * 4096, AUX_SC1);
      if (!__any((diff4(g[0], tag) | diff4(g[1], tag) | diff4(g[2], tag) | diff4(g[3], tag)) >> 24)) break;
      __builtin_amdgcn_s_sleep(1);
    }
    float sum[4];
#pragma unroll
    for (int k = 0; k < 4; ++k) {
      sum[k] = (dec_f(g[0][k]) + dec_f(g[1][k])) + (dec_f(g[2][k]) + dec_f(g[3][k]));
      sum[k] += dppf<ROR8>(sum[k]);
      sum[k] = sum16(sum[k], sum[k]);
      sum[k] = sum32(sum[k], sum[k]);
    }
    const auto ro = __builtin_amdgcn_make_buffer_rsrc(p.out, 0, 4 * 2 * DM * 2, RSRC_W3);
    __builtin_amdgcn_raw_buffer_store_b64(v2u{cvtpk(sum[0], sum[1]), cvtpk(sum[2], sum[3])}, ro, lane < ((ri & 12) == 12 ? 4 : 8) ? lane * 8 : OOB,
                                          ((ri & 3) * 2 * DM + (ri >> 6) * DM + ((ri >> 4) & 3) * 112 + ((ri >> 2) & 3) * 32) * 2, AUX_SC1);
    __builtin_amdgcn_raw_buffer_store_b32(tag, rws, (bid == 0 && lane == 0) ? 0 : OOB, WS_EPOCH, AUX_SC1);
  }
  asm volatile(".Ldec_code_end:");
}

}

extern "C" void kernel_launch(void* const* d_in, const int* in_sizes, int n_in, void* d_out, int out_size, void* d_ws, size_t ws_size,
                              hipStream_t stream) {
  Params p;
  p.x0 = d_in[0]; p.k0 = d_in[1]; p.v0 = d_in[2]; p.wq0 = d_in[3]; p.wo0 = d_in[4];
  p.x1 = d_in[5]; p.k1 = d_in[6]; p.v1 = d_in[7]; p.wq1 = d_in[8]; p.wo1 = d_in[9];
  p.out = d_out;
  p.ws = (unsigned char*)d_ws;
  decode_fused<<<256, 256, 0, stream>>>(p);
}
```

```cpp
#include <hip/hip_runtime.h>
#include <hip/hip_bf16.h>
#include <stdint.h>

namespace {

typedef unsigned v4u __attribute__((ext_vector_type(4)));
typedef unsigned v2u __attribute__((ext_vector_type(2)));
typedef unsigned long long v8p __attribute__((ext_vector_type(8)));
typedef unsigned long long v4p __attribute__((ext_vector_type(4)));
typedef float f32x4 __attribute__((ext_vector_type(4)));
typedef float f32x2 __attribute__((ext_vector_type(2)));
typedef short v4s __attribute__((ext_vector_type(4)));
typedef __bf16 bf16x2 __attribute__((ext_vector_type(2)));
typedef __bf16 bf16x8 __attribute__((ext_vector_type(8)));
#define LAS __attribute__((address_space(3)))
#define SB() __builtin_amdgcn_sched_barrier(0)
#define MEMFENCE() asm volatile("" ::: "memory")
#define KEEP(x) asm volatile("" :: "v"(x))
#define DRAIN() asm volatile("s_waitcnt vmcnt(0)" ::: "memory")
#define LDS_BARRIER() asm volatile("s_waitcnt lgkmcnt(0)\n\ts_barrier" ::: "memory")
#define NOW() ((unsigned)__builtin_amdgcn_s_memrealtime())

constexpr int DM = 448, NQ = 4096, SK = 8192, DH = 256;
constexpr unsigned REGION_BYTES = SK * DH * 2;

constexpr int PACE_GBS = 7820;
constexpr int PACE_OFF = 145;
constexpr int PACE_REL = 510;
constexpr int PUB_NOM = 204;
constexpr int EARLY = 44;
constexpr int EARLY_STEP = 4;
constexpr int SK_0 = 45;
constexpr int SK_K = 2;
constexpr int GAP_T = 0;
constexpr int SLIP_THR = 90;
constexpr int SLIP_N = 5;
constexpr int H_A = 8;
constexpr int H_P = 24;
constexpr int H_C = 32;
constexpr int H_S = 11;
constexpr int WO_E = 2;
constexpr int WO_T = (WO_E * 13);
constexpr int T_GATHER = 160;
constexpr int T_REDUCE = 316;
constexpr int PACE_PER = (int)(1048576.0 * 256.0 / (PACE_GBS * 10.0) + 0.5);
static_assert(H_C >= 32 && H_C <= 44 && H_P <= H_C && H_A % 4 == 0 && H_P % 4 == 0 && H_C % 4 == 0, "head schedule");

constexpr int AUX_NT = 2, AUX_SC1 = 16, RSRC_W3 = 0x00020000;
constexpr int OOB = 0x7fff0000;
constexpr unsigned SPIN_CAP = 1u << 18;
constexpr float SCL = 0.0625f * 1.4426950408889634f;

constexpr unsigned WS_EPOCH = 0;
constexpr unsigned WS_QG = 32768;
constexpr unsigned OG_STR = 2048 + 128;
constexpr unsigned WS_OG = WS_QG + 256 * 512;
constexpr unsigned WS_PG = WS_OG + 2 * 256 * OG_STR;
constexpr unsigned WS_END = WS_PG + 128 * 4096;

constexpr int WO_STR = 288;
constexpr int L_WO = 0;
constexpr int L_MRG = 128 * WO_STR;
constexpr int L_QP = L_MRG + 16384;
constexpr int L_Q = L_QP + 2048;
constexpr int L_O = L_Q + 2048;
constexpr int L_ML = L_O + 1024;
constexpr int L_SLIP = L_ML + 128;
constexpr int L_SC = L_SLIP + 16;
constexpr int L_BYTES = 84 * 1024;
static_assert(L_SC + 8 * 1024 <= L_BYTES && L_SC % 16 == 0, "lds map");

struct Params {
  const void *x0, *k0, *v0, *wq0, *wo0, *x1, *k1, *v1, *wq1, *wo1;
  void* out;
  unsigned char* ws;
};

__device__ __forceinline__ float dot2(unsigned a, unsigned b, float c) {
  return __builtin_amdgcn_fdot2_f32_bf16(__builtin_bit_cast(bf16x2, a), __builtin_bit_cast(bf16x2, b), c, false);
}
__device__ __forceinline__ unsigned pk_lo(unsigned a, unsigned b) { return __builtin_amdgcn_perm(b, a, 0x05040100u); }
__device__ __forceinline__ unsigned pk_hi(unsigned a, unsigned b) { return __builtin_amdgcn_perm(b, a, 0x07060302u); }
__device__ __forceinline__ unsigned cvtpk(float lo, float hi) {
  bf16x2 c = {(__bf16)lo, (__bf16)hi};
  return __builtin_bit_cast(unsigned, c);
}
template <int CTRL> __device__ __forceinline__ float dppf(float x) {
  return __builtin_bit_cast(float, __builtin_amdgcn_mov_dpp(__builtin_bit_cast(int, x), CTRL, 0xf, 0xf, true));
}
template <int CTRL> __device__ __forceinline__ unsigned dppu(unsigned x) {
  return (unsigned)__builtin_amdgcn_mov_dpp((int)x, CTRL, 0xf, 0xf, true);
}
constexpr int XOR1 = 0xB1, XOR2 = 0x4E, XOR7 = 0x141, ROR8 = 0x128, ROR4 = 0x124, ROR2 = 0x122, ROR1 = 0x121, BCAST = 0x150;
__device__ __forceinline__ float row_sum(float a) {
  a += dppf<ROR8>(a); a += dppf<ROR4>(a); a += dppf<ROR2>(a);
  return a + dppf<ROR1>(a);
}
__device__ __forceinline__ float row_max(float a) {
  a = fmaxf(a, dppf<ROR8>(a)); a = fmaxf(a, dppf<ROR4>(a)); a = fmaxf(a, dppf<ROR2>(a));
  return fmaxf(a, dppf<ROR1>(a));
}
__device__ __forceinline__ float sum16(float a, float b) {
  auto r = __builtin_amdgcn_permlane16_swap(__float_as_uint(a), __float_as_uint(b), false, false);
  return __uint_as_float(r[0]) + __uint_as_float(r[1]);
}
__device__ __forceinline__ float sum32(float a, float b) {
  auto r = __builtin_amdgcn_permlane32_swap(__float_as_uint(a), __float_as_uint(b), false, false);
  return __uint_as_float(r[0]) + __uint_as_float(r[1]);
}
__device__ __forceinline__ float max32(float a) {
  auto r = __builtin_amdgcn_permlane32_swap(__float_as_uint(a), __float_as_uint(a), false, false);
  return fmaxf(__uint_as_float(r[0]), __uint_as_float(r[1]));
}
constexpr unsigned TAG_INC = 0x35;
__device__ __forceinline__ unsigned enc_f(float f, unsigned tag) { return __builtin_amdgcn_alignbit(tag, __float_as_uint(f), 8); }
__device__ __forceinline__ float dec_f(unsigned d) { return __uint_as_float(d << 8); }
__device__ __forceinline__ unsigned diff4(v4u g, unsigned tag) {
  return ((g.x ^ (tag << 24)) | (g.y ^ ((tag + TAG_INC) << 24))) | ((g.z ^ ((tag + 2 * TAG_INC) << 24)) | (g.w ^ ((tag + 3 * TAG_INC) << 24)));
}

__device__ __forceinline__ unsigned diff2(unsigned a, unsigned b, unsigned t) { return __builtin_amdgcn_bitop3_b32(a, b, t, 0x7e); }
__device__ __forceinline__ unsigned diff16(const v4u (&g)[4], unsigned tag) {
  unsigned d[4];
#pragma unroll
  for (int k = 0; k < 4; ++k) d[k] = diff2(g[0][k], g[1][k], (tag + k * TAG_INC) << 24) | diff2(g[2][k], g[3][k], (tag + k * TAG_INC) << 24);
  return (d[0] | d[1]) | (d[2] | d[3]);
}

__device__ __forceinline__ void wait_until(unsigned t) {
  SB();
  asm volatile("1:\n\ts_memrealtime vcc\n\ts_waitcnt lgkmcnt(0)\n\ts_sub_u32 vcc_lo, vcc_lo, %0\n\ts_cmp_lt_i32 vcc_lo, 0\n\t"
               "s_cbranch_scc0 2f\n\ts_sleep 1\n\ts_branch 1b\n2:" :: "s"(__builtin_amdgcn_readfirstlane(t)) : "vcc", "scc", "memory");
  SB();
}
__device__ __forceinline__ void pace(unsigned t, int& lprev, unsigned slip_addr) {
  unsigned vt, slip, tgt;
  int lp = __builtin_amdgcn_readfirstlane(lprev);
  SB();
  asm volatile(
      "ds_read_b32 %0, %5\n\ts_memrealtime vcc\n\ts_waitcnt lgkmcnt(0)\n\t"
      "v_readfirstlane_b32 %1, %0\n\t"
      "s_add_u32 %2, %1, %4\n\t"
      "s_sub_u32 vcc_lo, vcc_lo, %2\n\t"
      "s_lshl_b32 %3, %3, 4\n\ts_sub_i32 vcc_hi, vcc_lo, %3\n\t"
      "s_cmp_gt_i32 vcc_hi, %7\n\t"
      "s_cbranch_scc0 3f\n\t"
      "s_add_u32 %1, %1, %6\n\ts_add_u32 %2, %2, %6\n\ts_sub_u32 vcc_lo, vcc_lo, %6\n\t"
      "v_mov_b32 %0, %1\n\tds_max_u32 %5, %0\n"
      "3:\n\ts_max_i32 %3, vcc_lo, 0\n"
      "1:\n\ts_cmp_lt_i32 vcc_lo, 0\n\ts_cbranch_scc0 2f\n\ts_sleep 1\n\t"
      "s_memrealtime vcc\n\ts_waitcnt lgkmcnt(0)\n\ts_sub_u32 vcc_lo, vcc_lo, %2\n\ts_branch 1b\n"
      "2:\n\ts_waitcnt lgkmcnt(0)"
      : "=&v"(vt), "=&s"(slip), "=&s"(tgt), "+s"(lp)
      : "s"(__builtin_amdgcn_readfirstlane(t)), "v"(slip_addr), "i"((SLIP_N * 4 * PACE_PER + 128) >> 8), "i"(SLIP_THR)
      : "vcc", "scc", "memory");
  lprev = __builtin_amdgcn_readfirstlane(lp);
  SB();
}


__global__ void __launch_bounds__(256, 2) decode_fused(Params args) {
  __shared__ __attribute__((aligned(16))) unsigned char smem[L_BYTES];
  LAS unsigned char* L = (LAS unsigned char*)smem;
  v8p arg_a;
  v4p arg_b;
  unsigned long long clk0;
  asm volatile("s_load_dwordx16 %0, %3, 0x0\n\ts_load_dwordx8 %1, %3, 0x40\n\ts_memrealtime %2"
               : "=&s"(arg_a), "=&s"(arg_b), "=&s"(clk0) : "s"(__builtin_amdgcn_kernarg_segment_ptr()));
  SB();
  const int bid = blockIdx.x, kvh = bid >> 5, b = (bid >> 3) & 3, sp = bid & 7;
  const int br = bid >> 7, j = (bid >> 3) & 15, kr = (sp + 4 * br) & 7, cb = j & 3, rb = j >> 2;
  const int tid = threadIdx.x, lane = tid & 63, w = __builtin_amdgcn_readfirstlane(tid >> 6);
  const int voff = lane * 16, koff = tid * 16;
  const bool b3 = lane & 8, b2 = lane & 4, b1 = lane & 2, b0 = lane & 1;
  LAS float* const sc = (LAS float*)(L + L_SC + tid * 4);

  unsigned cpf;
  {
    unsigned long long pc;
    unsigned len;
    asm volatile("s_getpc_b64 %0\n.Ldec_code_begin:\n\ts_mov_b32 %1, .Ldec_code_end-.Ldec_code_begin" : "=s"(pc), "=s"(len));
    cpf = __builtin_amdgcn_raw_buffer_load_b32(__builtin_amdgcn_make_buffer_rsrc((void*)pc, 0, len, RSRC_W3), tid < 8 ? tid * 128 : OOB, (bid >> 3) * 1024, 0);
  }
  const int row0 = 128 * w + 8 * (lane >> 2);
  int xoff = row0 < DM ? row0 * 2 : OOB, woff = row0 * (NQ * 2) + (lane & 3) * 16, wso = (kr * 512 + j * 32) * 2;
  int poffv = w == 3 && lane < 56 ? (lane >> 1) * (16 * NQ * 2) + (lane & 1) * 64 + kr * 1024 + j * (NQ * 2) : OOB;
  int eso = w == 0 ? WS_EPOCH : OOB;
  const int f2 = wso + 447 * (NQ * 2), f3 = WS_QG + bid * 512, f01 = (j & 1) ? wso : OOB, f23 = (j & 1) ? f3 : f2;
  int fso = (j & 2) ? f23 : f01;
  asm volatile("" : "+v"(xoff), "+v"(woff), "+v"(poffv));
  SB();
  asm volatile("s_waitcnt lgkmcnt(0)" : "+s"(arg_a), "+s"(arg_b), "+s"(clk0), "+s"(wso), "+s"(eso), "+s"(fso) :: "memory");
  SB();
  Params p;
  p.x0 = (void*)arg_a[0]; p.k0 = (void*)arg_a[1]; p.v0 = (void*)arg_a[2]; p.wq0 = (void*)arg_a[3]; p.wo0 = (void*)arg_a[4];
  p.x1 = (void*)arg_a[5]; p.k1 = (void*)arg_a[6]; p.v1 = (void*)arg_a[7]; p.wq1 = (void*)arg_b[0]; p.wo1 = (void*)arg_b[1];
  p.out = (void*)arg_b[2]; p.ws = (unsigned char*)arg_b[3];
  unsigned t0 = (unsigned)clk0;
  const auto rws = __builtin_amdgcn_make_buffer_rsrc(p.ws, 0, WS_END, RSRC_W3);

  v4u X[4], W[8], X0[16], X1[16], R[16];
  unsigned pfw, ft;
  unsigned ep;
  {
    const auto rx = __builtin_amdgcn_make_buffer_rsrc((void*)(br ? p.x1 : p.x0), 0, 4 * DM * 2, RSRC_W3);
    const auto rwq = __builtin_amdgcn_make_buffer_rsrc((void*)(br ? p.wq1 : p.wq0), 0, DM * NQ * 2, RSRC_W3);
    {
      const size_t wqp = (size_t)(br ? p.wq1 : p.wq0), a = (j & 3) == 3 ? (size_t)p.ws : wqp;
      ft = __builtin_amdgcn_raw_buffer_load_b32(__builtin_amdgcn_make_buffer_rsrc((void*)a, 0, DM * NQ * 2, RSRC_W3), 0, fso, AUX_NT);
      SB();
    }
#pragma unroll
    for (int i = 0; i < 4; ++i) X[i] = __builtin_amdgcn_raw_buffer_load_b128(rx, xoff + i * DM * 2, 0, 0);
    pfw = __builtin_amdgcn_raw_buffer_load_b32(rwq, poffv, (384u - (unsigned)(size_t)(br ? p.wq1 : p.wq0)) & 896u, AUX_NT);
    SB();
#pragma unroll
    for (int n = 0; n < 8; ++n) W[n] = __builtin_amdgcn_raw_buffer_load_b128(rwq, woff, wso + n * (NQ * 2), AUX_NT);
    SB();
    ep = __builtin_amdgcn_raw_buffer_load_b32(rws, 0, eso, AUX_SC1);
    SB();
  }
  asm volatile("" : "+s"(p.wo0), "+s"(p.wo1), "+s"(p.v1));
  const size_t reg_off = (size_t)(b * 8 + kvh) * REGION_BYTES + sp * (REGION_BYTES / 8);
  const char* const wop = (const char*)(br ? p.wo1 : p.wo0) + (kr * 512 + rb * 128) * (DM * 2) + cb * 224;
#define LDU(rs, i) __builtin_amdgcn_raw_buffer_load_b128(rs, koff, (i) * 4096, AUX_NT)
  const int poff = PACE_OFF * 256 + (((b * 8 + kvh) * 19) & 31) * (4 * PACE_PER / 32);
  const auto rk0 = __builtin_amdgcn_make_buffer_rsrc((char*)p.k0 + reg_off, 0, 48 * 4096, RSRC_W3);
#define ISSUE(lo, hi)                                                                     \
  _Pragma("unroll") for (int i_ = (lo); i_ < (hi) && i_ < 48; ++i_) {                     \
    if (i_ >= H_A + 4 && (i_ & 3) == 0) wait_until(t0 + ((i_ * PACE_PER + poff) >> 8));   \
    if (i_ < 16) X0[i_ & 15] = LDU(rk0, i_);                                              \
    else if (i_ < 32) X1[i_ & 15] = LDU(rk0, i_);                                         \
    else R[i_ & 15] = LDU(rk0, i_);                                                       \
    SB();                                                                                 \
  }
  ISSUE(0, H_A)

  {
    float a[4][8];
#pragma unroll
    for (int bb = 0; bb < 4; ++bb)
#pragma unroll
      for (int c = 0; c < 8; ++c) a[bb][c] = 0.f;
#pragma unroll
    for (int t = 0; t < 4; ++t) {
#pragma unroll
      for (int k = 0; k < 4; ++k) {
        const unsigned lo = pk_lo(W[2 * t][k], W[2 * t + 1][k]), hi = pk_hi(W[2 * t][k], W[2 * t + 1][k]);
#pragma unroll
        for (int bb = 0; bb < 4; ++bb) {
          a[bb][2 * k] = dot2(lo, X[bb][t], a[bb][2 * k]);
          a[bb][2 * k + 1] = dot2(hi, X[bb][t], a[bb][2 * k + 1]);
        }
      }
      SB();
      ISSUE(H_A + t, H_A + t + 1)
    }
    float y[8];
#pragma unroll
    for (int c = 0; c < 8; ++c) {
      y[c] = sum16(sum32(a[0][c], a[2][c]), sum32(a[1][c], a[3][c]));
      y[c] += dppf<ROR8>(y[c]);
      y[c] += dppf<ROR4>(y[c]);
    }
    LAS f32x4* qp = (LAS f32x4*)(L + L_QP + w * 512 + (lane >> 4) * 128 + (lane & 3) * 32);
    qp[0] = f32x4{y[0], y[1], y[2], y[3]};
    qp[1] = f32x4{y[4], y[5], y[6], y[7]};
  }
  *(LAS unsigned*)(L + (w == 0 ? L_ML : L_O)) = ep;
  LDS_BARRIER();
  const unsigned tag = __builtin_amdgcn_readfirstlane(*(volatile LAS unsigned*)(L + L_ML)) + 1u;
  if (w == 0) {
    const unsigned tt = ((NOW() >> 2) & 0xffu) | tag << 8;
    const LAS f32x4* qp = (const LAS f32x4*)(L + L_QP + (lane & 31) * 16);
    const f32x4 q = (qp[0] + qp[32]) + (qp[64] + qp[96]);
    const unsigned lo = cvtpk(q.x, q.y), hi = cvtpk(q.z, q.w);
    __builtin_amdgcn_raw_buffer_store_b128(v4u{pk_lo(lo, tt), __builtin_amdgcn_alignbit(tt + (TAG_INC << 8), lo, 16), pk_lo(hi, tt + (2 * TAG_INC << 8)),
                                               __builtin_amdgcn_alignbit(tt + (3 * TAG_INC << 8), hi, 16)},
                                           rws, lane < 32 ? voff : OOB, WS_QG + (kr * 32 + br * 16 + j) * 512, AUX_SC1);
  }
  SB();
  ISSUE(H_A + 4, H_A + 8)
  unsigned tch;
  {
    const size_t a12 = w == 1 ? (size_t)p.v0 : (size_t)p.k1, a03 = w == 3 ? (size_t)p.v1 : (size_t)wop;
    const size_t a = (w == 1 || w == 2 ? a12 : a03) + (w ? reg_off : 0);
    tch = __builtin_amdgcn_raw_buffer_load_b32(__builtin_amdgcn_make_buffer_rsrc((void*)a, 0, 65536, RSRC_W3), lane == 0 ? 0 : OOB,
                                               ((bid * 3 + w) & 63) * 896, AUX_NT);
    SB();
  }
  ISSUE(H_A + 8, H_P)
  int budget;
  {
    const int qg_off = WS_QG + kvh * 32 * 512 + b * 128, qg_v = (tid >> 3) * 512 + (tid & 7) * 16;
    const int ck_v = w == 0 ? (lane & 15) * (16 * 512) + 4 * 512 : OOB;
    const unsigned ck_mask = w == 0 ? ~0u : 0u;
    v4u qg = __builtin_amdgcn_raw_buffer_load_b128(rws, qg_v, qg_off, AUX_SC1);
    v4u ck = __builtin_amdgcn_raw_buffer_load_b128(rws, ck_v, WS_QG, AUX_SC1);
    SB();
    ISSUE(H_P, H_C)
    KEEP(cpf);
    KEEP(ft);
    KEEP(pfw);
    if (__any((diff4(qg, tag) | (diff4(ck, tag) & ck_mask)) >> 24)) {
#pragma unroll 1
      for (unsigned spins = 0; spins < SPIN_CAP; ++spins) {
        MEMFENCE();
        qg = __builtin_amdgcn_raw_buffer_load_b128(rws, qg_v, qg_off, AUX_SC1);
        ck = __builtin_amdgcn_raw_buffer_load_b128(rws, ck_v, WS_QG, AUX_SC1);
        if (!__any((diff4(qg, tag) | (diff4(ck, tag) & ck_mask)) >> 24)) break;
        __builtin_amdgcn_s_sleep(2);
      }
    }
    *(LAS v2u*)(L + L_Q + tid * 8) = v2u{pk_lo(qg.x, qg.y), pk_lo(qg.z, qg.w)};
    const unsigned ref = (t0 + PUB_NOM) & ~3u;
    const float d = (float)(int)(signed char)((ck.x >> 16) - (ref >> 2));
    const int mean = (int)row_sum(d) >> 2, skew = (int)row_max(d) * 4 - mean;
    *(LAS v4u*)(L + (w == 0 ? L_SLIP : L_O)) = v4u{0u, ref + (unsigned)mean - PUB_NOM, (unsigned)skew, 0u};
  }
  LDS_BARRIER();
  {
    const v4u an = *(const LAS v4u*)(L + L_SLIP);
    t0 = __builtin_amdgcn_readfirstlane(t0 + min(max((int)(an.y - t0), -150), 400));
    budget = __builtin_amdgcn_readfirstlane(EARLY + ((min(max((int)an.z - SK_0, 0), 200) * SK_K) >> 2));
  }
  v4u q0 = *(const LAS v4u*)(L + L_Q + (lane & 31) * 16), q1 = *(const LAS v4u*)(L + L_Q + 512 + (lane & 31) * 16);

  float acc[2][8], m = -INFINITY, l = 0.f;
#pragma unroll
  for (int k = 0; k < 8; ++k) acc[0][k] = acc[1][k] = 0.f;
#define QK_STEP(KR, i)                                                 \
  {                                                                    \
    float s0 = dot2(KR[i][0], q0[0], 0.f), s1 = dot2(KR[i][0], q1[0], 0.f); \
    s0 = dot2(KR[i][1], q0[1], s0); s1 = dot2(KR[i][1], q1[1], s1);    \
    s0 = dot2(KR[i][2], q0[2], s0); s1 = dot2(KR[i][2], q1[2], s1);    \
    s0 = dot2(KR[i][3], q0[3], s0); s1 = dot2(KR[i][3], q1[3], s1);    \
    y[i] = sum16(s0, s1);                                              \
  }
#define TR_STEP(H, CTRL, BIT)                                          \
  _Pragma("unroll") for (int i = 0; i < (H); ++i) {                    \
    const float keep = (BIT) ? y[i + (H)] : y[i], send = (BIT) ? y[i] : y[i + (H)]; \
    y[i] = keep + dppf<CTRL>(send);                                    \
  }
#define SCORES(slot)                                                   \
  TR_STEP(8, ROR8, b3) TR_STEP(4, XOR7, b2) TR_STEP(2, XOR2, b1) TR_STEP(1, XOR1, b0) \
  m = fmaxf(m, y[0]);                                                  \
  sc[(slot) * 256] = y[0];
#define PROBS(slot)                                                                     \
  const float pp = __builtin_amdgcn_exp2f((sc[(slot) * 256] - m) * SCL);                \
  l += pp;                                                                              \
  const unsigned ppk = cvtpk(pp, dppf<XOR1>(pp));        \
  const auto ph = __builtin_amdgcn_permlane16_swap(ppk, ppk, false, false);
#define PV_STEP(t)                                                     \
  {                                                                    \
    const unsigned p0 = dppu<BCAST + 2 * (t)>(ph[0]), p1 = dppu<BCAST + 2 * (t)>(ph[1]); \
    _Pragma("unroll") for (int k = 0; k < 4; ++k) {                    \
      const unsigned lo = pk_lo(R[2 * (t)][k], R[2 * (t) + 1][k]), hi = pk_hi(R[2 * (t)][k], R[2 * (t) + 1][k]); \
      acc[0][2 * k] = dot2(lo, p0, acc[0][2 * k]); acc[0][2 * k + 1] = dot2(hi, p0, acc[0][2 * k + 1]); \
      acc[1][2 * k] = dot2(lo, p1, acc[1][2 * k]); acc[1][2 * k + 1] = dot2(hi, p1, acc[1][2 * k + 1]); \
    }                                                                  \
  }
#define DUMP(brn)                                                      \
  {                                                                    \
    float o[8];                                                        \
    _Pragma("unroll") for (int k = 0; k < 8; ++k) o[k] = sum32(acc[0][k], acc[1][k]);     \
    LAS f32x4* mp = (LAS f32x4*)(L + L_MRG + (brn) * 8192 + tid * 32); \
    mp[0] = f32x4{o[0], o[1], o[2], o[3]};                             \
    mp[1] = f32x4{o[4], o[5], o[6], o[7]};                             \
  }
#define DUMP_ML(brn)                                                   \
  l = row_sum(l);                                                      \
  l = sum32(l, l);                                                     \
  *(LAS f32x2*)(L + L_ML + (brn) * 64 + w * 16 + ((lane >> 4) & 1) * 8) = f32x2{m, l};
  {
    float y[16];
#pragma unroll
    for (int i = 0; i < 16; ++i) {
      if (i % H_S == 0) { SB(); ISSUE(H_C + (i / H_S) * 4, H_C + (i / H_S) * 4 + 4) }
      QK_STEP(X0, i)
    }
    SCORES(0)
#pragma unroll
    for (int i = 0; i < 16; ++i) {
      if ((i + 16) % H_S == 0) { SB(); ISSUE(H_C + ((i + 16) / H_S) * 4, H_C + ((i + 16) / H_S) * 4 + 4) }
      QK_STEP(X1, i)
    }
    SCORES(1)
    SB();
  }
  ISSUE(H_C + (31 / H_S + 1) * 4, 48)
  v4u WO[8];
  const int ooff = (lane & 15) < 14 ? (lane >> 4) * (DM * 2) + (lane & 15) * 16 : OOB;
#define WO_LOADS(lo, hi)                                                                  \
  SB();                                                                                   \
  _Pragma("unroll") for (int n = (lo); n < (hi); ++n)                                     \
    WO[n] = __builtin_amdgcn_raw_buffer_load_b128(__builtin_amdgcn_make_buffer_rsrc((void*)wop, 0, 127 * DM * 2 + 224, RSRC_W3), ooff, \
                                                  (32 * w + 4 * n) * (DM * 2), AUX_NT);   \
  SB();
  wait_until(t0 + ((48 * PACE_PER + poff) >> 8));
  KEEP(tch);
  WO_LOADS(0, WO_E)
  t0 += WO_T;
  {
    const unsigned slip_addr = (unsigned)(unsigned long)(L + L_SLIP);
    int lprev = 1 << 20;
    int xt = 48 * PACE_PER + poff;
#define BURST(g)                                                       \
  { const int inc_ = min(budget, EARLY_STEP); t0 += inc_; budget -= inc_; } \
  pace(t0 + (min(xt, PACE_REL * PACE_PER + PACE_OFF * 256) >> 8), lprev, slip_addr); \
  xt += 4 * PACE_PER;                                                  \
  _Pragma("unroll") for (int u_ = 4 * (g); u_ < 4 * (g) + 4; ++u_) { R[u_] = LDU(rn, u_); SB(); }
    const char *kp = (const char*)p.k0 + reg_off, *vp = (const char*)p.v0 + reg_off;
    int g = 2;
#pragma unroll 1
    for (int grp = 0;; ++grp) {
#pragma unroll 1
      do {
        const auto rn = __builtin_amdgcn_make_buffer_rsrc((void*)(g < 7 ? kp + (g + 1) * 65536 : vp), 0, 65536, RSRC_W3);
        float y[16];
#pragma unroll
        for (int i = 0; i < 16; ++i) {
          QK_STEP(R, i)
          if ((i & 3) == 3) { BURST(i >> 2) }
        }
        SCORES(g)
        SB();
      } while (++g < 8);
      m = max32(row_max(m));
      kp = (const char*)p.k1 + reg_off;
      budget += GAP_T;
      g = 0;
#pragma unroll 1
      do {
        const auto rn = __builtin_amdgcn_make_buffer_rsrc((void*)(g < 7 ? vp + (g + 1) * 65536 : kp), 0, 65536, RSRC_W3);
        PROBS(g)
        PV_STEP(0) PV_STEP(1) BURST(0) PV_STEP(2) PV_STEP(3) BURST(1) PV_STEP(4) PV_STEP(5) BURST(2) PV_STEP(6) PV_STEP(7) BURST(3)
      } while (++g < 8 - grp);
      if (grp) break;
      DUMP(0)
      DUMP_ML(0)
#pragma unroll
      for (int k = 0; k < 8; ++k) acc[0][k] = acc[1][k] = 0.f;
      m = -INFINITY;
      l = 0.f;
      q0 = *(const LAS v4u*)(L + L_Q + 1024 + (lane & 31) * 16);
      q1 = *(const LAS v4u*)(L + L_Q + 1536 + (lane & 31) * 16);
      vp = (const char*)p.v1 + reg_off;
      budget += GAP_T;
      g = 0;
    }
  }
  const unsigned t_end = t0 + __builtin_amdgcn_readfirstlane(*(volatile LAS unsigned*)(L + L_SLIP)) + ((512 * PACE_PER + PACE_OFF * 256) >> 8);
  int a_ml = L_ML + (w >> 1) * 64 + (w & 1) * 8, a_mrg = L_MRG + (w >> 1) * 8192 + (w & 1) * 1024 + voff;
  int og = WS_OG + ((w >> 1) * 256 + bid) * OG_STR + (w & 1) * 1024, ogm = WS_OG + ((w >> 1) * 256 + bid) * OG_STR + 2048 + (w & 1) * 16;
  unsigned tg1 = tag + TAG_INC, tg2 = tag + 2 * TAG_INC, tg3 = tag + 3 * TAG_INC;
  asm volatile("" : "+v"(a_ml), "+v"(a_mrg));
  asm volatile("" : "+s"(og), "+s"(ogm), "+s"(tg1), "+s"(tg2), "+s"(tg3));
#define ENC4(f) v4u{enc_f(f.x, tag), enc_f(f.y, tg1), enc_f(f.z, tg2), enc_f(f.w, tg3)}
  {
    PROBS(7)
    DUMP_ML(1)
    SB();
    PV_STEP(0) PV_STEP(1) PV_STEP(2) PV_STEP(3) PV_STEP(4) PV_STEP(5) PV_STEP(6) PV_STEP(7) WO_LOADS(WO_E, (WO_E + 9) / 2)
  }

  DUMP(1)
  LDS_BARRIER();
  WO_LOADS((WO_E + 9) / 2, 8)
  {
    f32x2 ml[4];
    f32x4 ov[4];
#pragma unroll
    for (int i = 0; i < 4; ++i) {
      ml[i] = *(const LAS f32x2*)(L + a_ml + i * 16);
      ov[i] = *(const LAS f32x4*)(L + a_mrg + i * 2048);
    }
    const float M = fmaxf(fmaxf(ml[0].x, ml[1].x), fmaxf(ml[2].x, ml[3].x));
    f32x4 o = {0.f, 0.f, 0.f, 0.f};
    float ls = 0.f;
#pragma unroll
    for (int i = 0; i < 4; ++i) {
      const float e = __builtin_amdgcn_exp2f((ml[i].x - M) * SCL);
      o += e * ov[i];
      ls = fmaf(e, ml[i].y, ls);
    }
    __builtin_amdgcn_raw_buffer_store_b128(ENC4(o), rws, voff, og, AUX_SC1);
    __builtin_amdgcn_raw_buffer_store_b128(v4u{enc_f(M, tag), (__float_as_uint(M) & 0xffu) | tag << 8, enc_f(ls, tag), tag}, rws, lane == 0 ? 0 : OOB, ogm, AUX_SC1);
  }
  int og_v = (lane >> 5) * OG_STR + (lane & 31) * 16;
  int ml_v = (((lane & 3) * 2 + (lane >> 5)) ^ ((lane >> 2) & 1)) * OG_STR;
  int a_ow = L_O + w * 256 + (lane & 31) * 8 + (lane >> 5) * 4, a_or = L_O + (lane & 3) * 256 + (lane >> 4) * 16;
  int pg_v = b3 ? OOB : (lane & 3) * 4096 + (lane & 4) * 16 + (lane >> 4) * 16;
  int go[5], pg_s = WS_PG + ((br * 4 + cb) * 4 + w) * 16384 + (kr * 4 + rb) * 128;
#pragma unroll
  for (int s = 0; s < 5; ++s) go[s] = WS_OG + (br * 256 + kr * 32 + w * 8) * OG_STR + (s < 4 ? rb * 512 + s * 2 * OG_STR : 2048 + (rb >> 1) * 16);
  asm volatile("" : "+v"(og_v), "+v"(ml_v), "+v"(a_ow), "+v"(a_or), "+v"(pg_v));
  asm volatile("" : "+s"(go[0]), "+s"(go[1]), "+s"(go[2]), "+s"(go[3]), "+s"(go[4]), "+s"(pg_s));
  wait_until(t_end + T_GATHER);

  v4u BW[2][4];
#define BW_READS()                                                                        \
  _Pragma("unroll") for (int ks = 0; ks < 4; ++ks) _Pragma("unroll") for (int c = 0; c < 2; ++c) { \
    LAS unsigned char* a = L + L_WO + (32 * ks + 8 * (lane >> 4) + ((lane >> 2) & 3)) * WO_STR + (2 * w + c) * 32 + (lane & 3) * 8; \
    const v2u r0 = __builtin_bit_cast(v2u, __builtin_amdgcn_ds_read_tr16_b64_v4i16((LAS v4s*)a));  \
    const v2u r1 = __builtin_bit_cast(v2u, __builtin_amdgcn_ds_read_tr16_b64_v4i16((LAS v4s*)(a + 4 * WO_STR))); \
    BW[c][ks] = v4u{r0.x, r0.y, r1.x, r1.y};                                              \
  }
  {
    v4u g[4], gm;
#define G_BAD() (((diff2(gm.x, gm.z, tag << 24) | diff16(g, tag)) >> 24) | \
                 ((gm.y ^ (tag << 8)) >> 8) | (gm.w ^ tag))
#define GATHER()                                                                                          \
  gm = __builtin_amdgcn_raw_buffer_load_b128(rws, ml_v, go[4], AUX_SC1);                                  \
  _Pragma("unroll") for (int s = 0; s < 4; ++s) g[s] = __builtin_amdgcn_raw_buffer_load_b128(rws, og_v, go[s], AUX_SC1);
    GATHER()
    SB();
#pragma unroll
    for (int n = 0; n < 8; ++n) *(LAS v4u*)(L + L_WO + (32 * w + 4 * n + (lane >> 4)) * WO_STR + (lane & 15) * 16) = WO[n];
    LDS_BARRIER();
    BW_READS()
    SB();
    if (__any(G_BAD())) {
#pragma unroll 1
      for (unsigned spins = 0; spins < SPIN_CAP; ++spins) {
        MEMFENCE();
        GATHER()
        if (!__any(G_BAD())) break;
        __builtin_amdgcn_s_sleep(1);
      }
    }
    const float Ms = __uint_as_float(gm.x << 8 | (gm.y & 0xffu)), Ls = dec_f(gm.z);
    float Mx = fmaxf(Ms, dppf<XOR1>(Ms));
    Mx = fmaxf(Mx, dppf<XOR2>(Mx));
    Mx = fmaxf(Mx, dppf<XOR7>(Mx));
    const float e = __builtin_amdgcn_exp2f((Ms - Mx) * SCL);
    float Lt = e * Ls;
    Lt += dppf<XOR1>(Lt);
    Lt += dppf<XOR2>(Lt);
    Lt += dppf<XOR7>(Lt);
    const float c = e * __builtin_amdgcn_rcpf(Lt);
    float o0 = 0.f, o1 = 0.f, o2 = 0.f, o3 = 0.f;
#define MRG(s) { const float cs = dppf<BCAST + s>(c); o0 = fmaf(cs, dec_f(g[s].x), o0); o1 = fmaf(cs, dec_f(g[s].y), o1); o2 = fmaf(cs, dec_f(g[s].z), o2); o3 = fmaf(cs, dec_f(g[s].w), o3); }
    MRG(0) MRG(1) MRG(2) MRG(3)
    *(LAS unsigned*)(L + a_ow) = cvtpk(sum32(o0, o2), sum32(o1, o3));
  }
  LDS_BARRIER();
  {
    v4u o[4];
#pragma unroll
    for (int ks = 0; ks < 4; ++ks) o[ks] = *(const LAS v4u*)(L + a_or + ks * 64);
    SB();
    f32x4 d0 = {0.f, 0.f, 0.f, 0.f}, d1 = {0.f, 0.f, 0.f, 0.f};
#pragma unroll
    for (int ks = 0; ks < 4; ++ks) {
      d0 = __builtin_amdgcn_mfma_f32_16x16x32_bf16(__builtin_bit_cast(bf16x8, BW[0][ks]), __builtin_bit_cast(bf16x8, o[ks]), d0, 0, 0, 0);
      d1 = __builtin_amdgcn_mfma_f32_16x16x32_bf16(__builtin_bit_cast(bf16x8, BW[1][ks]), __builtin_bit_cast(bf16x8, o[ks]), d1, 0, 0, 0);
    }
    const f32x4 d = b2 ? d1 : d0;
    __builtin_amdgcn_raw_buffer_store_b128(ENC4(d), rws, pg_v, pg_s, AUX_SC1);
  }

  if (w == 3 && (bid & 1) == 0) {
    const int ri = bid >> 1;
    v4u g[4];
    int o_v = lane < ((ri & 12) == 12 ? 4 : 8) ? lane * 8 : OOB;
    int ps = WS_PG + ri * 4096, o_s = ((ri & 3) * 2 * DM + (ri >> 6) * DM + ((ri >> 4) & 3) * 112 + ((ri >> 2) & 3) * 32) * 2;
    asm volatile("" : "+v"(o_v));
    asm volatile("" : "+s"(ps), "+s"(o_s));
    DRAIN();
    wait_until(t_end + T_REDUCE);
#pragma unroll 1
    for (unsigned spins = 0; spins < SPIN_CAP; ++spins) {
      MEMFENCE();
#pragma unroll
      for (int s = 0; s < 4; ++s) g[s] = __builtin_amdgcn_raw_buffer_load_b128(rws, voff + s * 1024, ps, AUX_SC1);
      if (!__any(diff16(g, tag) >> 24)) break;
      __builtin_amdgcn_s_sleep(1);
    }
    float sum[4];
#pragma unroll
    for (int k = 0; k < 4; ++k) {
      sum[k] = (dec_f(g[0][k]) + dec_f(g[1][k])) + (dec_f(g[2][k]) + dec_f(g[3][k]));
      sum[k] += dppf<ROR8>(sum[k]);
      sum[k] = sum16(sum[k], sum[k]);
      sum[k] = sum32(sum[k], sum[k]);
    }
    const auto ro = __builtin_amdgcn_make_buffer_rsrc(p.out, 0, 4 * 2 * DM * 2, RSRC_W3);
    __builtin_amdgcn_raw_buffer_store_b64(v2u{cvtpk(sum[0], sum[1]), cvtpk(sum[2], sum[3])}, ro, o_v, o_s, AUX_SC1);
    __builtin_amdgcn_raw_buffer_store_b32(tag, rws, (bid == 0 && lane == 0) ? 0 : OOB, WS_EPOCH, AUX_SC1);
  }
  asm volatile(".Ldec_code_end:");
}

}

extern "C" void kernel_launch(void* const* d_in, const int* in_sizes, int n_in, void* d_out, int out_size, void* d_ws, size_t ws_size,
                              hipStream_t stream) {
  Params p;
  p.x0 = d_in[0]; p.k0 = d_in[1]; p.v0 = d_in[2]; p.wq0 = d_in[3]; p.wo0 = d_in[4];
  p.x1 = d_in[5]; p.k1 = d_in[6]; p.v1 = d_in[7]; p.wq1 = d_in[8]; p.wo1 = d_in[9];
  p.out = d_out;
  p.ws = (unsigned char*)d_ws;
  decode_fused<<<256, 256, 0, stream>>>(p);
}
```

```cpp
#include <hip/hip_runtime.h>
#include <hip/hip_bf16.h>
#include <stdint.h>

namespace {

typedef unsigned v4u __attribute__((ext_vector_type(4)));
typedef unsigned v2u __attribute__((ext_vector_type(2)));
typedef unsigned long long v8p __attribute__((ext_vector_type(8)));
typedef unsigned long long v4p __attribute__((ext_vector_type(4)));
typedef float f32x4 __attribute__((ext_vector_type(4)));
typedef float f32x2 __attribute__((ext_vector_type(2)));
typedef short v4s __attribute__((ext_vector_type(4)));
typedef __bf16 bf16x2 __attribute__((ext_vector_type(2)));
typedef __bf16 bf16x8 __attribute__((ext_vector_type(8)));
#define LAS __attribute__((address_space(3)))
#define SB() __builtin_amdgcn_sched_barrier(0)
#define MEMFENCE() asm volatile("" ::: "memory")
#define KEEP(x) asm volatile("" :: "v"(x))
#define DRAIN() asm volatile("s_waitcnt vmcnt(0)" ::: "memory")
#define LDS_BARRIER() asm volatile("s_waitcnt lgkmcnt(0)\n\ts_barrier" ::: "memory")
#define NOW() ((unsigned)__builtin_amdgcn_s_memrealtime())

constexpr int DM = 448, NQ = 4096, SK = 8192, DH = 256;
constexpr unsigned REGION_BYTES = SK * DH * 2;

constexpr int PACE_GBS = 7820;
constexpr int PACE_OFF = 145;
constexpr int PUB_NOM = 204;
constexpr int EARLY = 44;
constexpr int EARLY_STEP = 4;
constexpr int SK_0 = 90;
constexpr int SK_K = 2;
constexpr int SLIP_THR = 90;
constexpr int SLIP_N = 5;
constexpr int H_A = 8;
constexpr int H_P = 24;
constexpr int H_C = 32;
constexpr int H_S = 11;
constexpr int G_SPREAD = 50;
constexpr int WO_S = 4;
constexpr int T_GATHER = 160;
constexpr int T_REDUCE = 313;
constexpr int PACE_PER = (int)(1048576.0 * 256.0 / (PACE_GBS * 10.0) + 0.5);
static_assert(H_C >= 32 && H_C <= 44 && H_P <= H_C && H_A % 4 == 0 && H_P % 4 == 0 && H_C % 4 == 0, "head schedule");

constexpr int AUX_NT = 2, AUX_SC1 = 16, RSRC_W3 = 0x00020000;
constexpr int OOB = 0x7fff0000;
constexpr unsigned SPIN_CAP = 1u << 18;
constexpr float SCL = 0.0625f * 1.4426950408889634f;

constexpr unsigned WS_EPOCH = 0;
constexpr unsigned WS_FREE = 4096;
constexpr unsigned WS_QG = 32768;
constexpr unsigned OG_STR = 2048 + 128;
constexpr unsigned WS_OG = WS_QG + 256 * 512;
constexpr unsigned WS_PG = WS_OG + 2 * 256 * OG_STR;
constexpr unsigned WS_END = WS_PG + 128 * 4096;

constexpr int WO_STR = 288;
constexpr int L_WO = 0;
constexpr int L_MRG = 128 * WO_STR;
constexpr int L_QP = L_MRG + 16384;
constexpr int L_Q = L_QP + 2048;
constexpr int L_O = L_Q + 2048;
constexpr int L_ML = L_O + 1024;
constexpr int L_SLIP = L_ML + 128;
constexpr int L_SC = L_SLIP + 16;
constexpr int L_BYTES = 84 * 1024;
static_assert(L_SC + 8 * 1024 <= L_BYTES && L_SC % 16 == 0, "lds map");

struct Params {
  const void *x0, *k0, *v0, *wq0, *wo0, *x1, *k1, *v1, *wq1, *wo1;
  void* out;
  unsigned char* ws;
};

__device__ __forceinline__ float dot2(unsigned a, unsigned b, float c) {
  return __builtin_amdgcn_fdot2_f32_bf16(__builtin_bit_cast(bf16x2, a), __builtin_bit_cast(bf16x2, b), c, false);
}
__device__ __forceinline__ unsigned pk_lo(unsigned a, unsigned b) { return __builtin_amdgcn_perm(b, a, 0x05040100u); }
__device__ __forceinline__ unsigned pk_hi(unsigned a, unsigned b) { return __builtin_amdgcn_perm(b, a, 0x07060302u); }
__device__ __forceinline__ unsigned cvtpk(float lo, float hi) {
  bf16x2 c = {(__bf16)lo, (__bf16)hi};
  return __builtin_bit_cast(unsigned, c);
}
template <int CTRL> __device__ __forceinline__ float dppf(float x) {
  return __builtin_bit_cast(float, __builtin_amdgcn_mov_dpp(__builtin_bit_cast(int, x), CTRL, 0xf, 0xf, true));
}
template <int CTRL> __device__ __forceinline__ unsigned dppu(unsigned x) {
  return (unsigned)__builtin_amdgcn_mov_dpp((int)x, CTRL, 0xf, 0xf, true);
}
constexpr int XOR1 = 0xB1, XOR2 = 0x4E, XOR7 = 0x141, ROR8 = 0x128, ROR4 = 0x124, ROR2 = 0x122, ROR1 = 0x121, BCAST = 0x150;
__device__ __forceinline__ float row_sum(float a) {
  a += dppf<ROR8>(a); a += dppf<ROR4>(a); a += dppf<ROR2>(a);
  return a + dppf<ROR1>(a);
}
__device__ __forceinline__ float row_max(float a) {
  a = fmaxf(a, dppf<ROR8>(a)); a = fmaxf(a, dppf<ROR4>(a)); a = fmaxf(a, dppf<ROR2>(a));
  return fmaxf(a, dppf<ROR1>(a));
}
__device__ __forceinline__ float sum16(float a, float b) {
  auto r = __builtin_amdgcn_permlane16_swap(__float_as_uint(a), __float_as_uint(b), false, false);
  return __uint_as_float(r[0]) + __uint_as_float(r[1]);
}
__device__ __forceinline__ float sum32(float a, float b) {
  auto r = __builtin_amdgcn_permlane32_swap(__float_as_uint(a), __float_as_uint(b), false, false);
  return __uint_as_float(r[0]) + __uint_as_float(r[1]);
}
__device__ __forceinline__ float max32(float a) {
  auto r = __builtin_amdgcn_permlane32_swap(__float_as_uint(a), __float_as_uint(a), false, false);
  return fmaxf(__uint_as_float(r[0]), __uint_as_float(r[1]));
}
constexpr unsigned TAG_INC = 0x35;
__device__ __forceinline__ unsigned enc_f(float f, unsigned tag) { return __builtin_amdgcn_alignbit(tag, __float_as_uint(f), 8); }
__device__ __forceinline__ float dec_f(unsigned d) { return __uint_as_float(d << 8); }
__device__ __forceinline__ unsigned diff4(v4u g, unsigned tag) {
  return ((g.x ^ (tag << 24)) | (g.y ^ ((tag + TAG_INC) << 24))) | ((g.z ^ ((tag + 2 * TAG_INC) << 24)) | (g.w ^ ((tag + 3 * TAG_INC) << 24)));
}

__device__ __forceinline__ unsigned diff2(unsigned a, unsigned b, unsigned t) { return __builtin_amdgcn_bitop3_b32(a, b, t, 0x7e); }
__device__ __forceinline__ unsigned diff16(const v4u (&g)[4], unsigned tag) {
  unsigned d[4];
#pragma unroll
  for (int k = 0; k < 4; ++k) d[k] = diff2(g[0][k], g[1][k], (tag + k * TAG_INC) << 24) | diff2(g[2][k], g[3][k], (tag + k * TAG_INC) << 24);
  return (d[0] | d[1]) | (d[2] | d[3]);
}

__device__ __forceinline__ void wait_until(unsigned t) {
  SB();
  asm volatile("1:\n\ts_memrealtime vcc\n\ts_waitcnt lgkmcnt(0)\n\ts_sub_u32 vcc_lo, vcc_lo, %0\n\ts_cmp_lt_i32 vcc_lo, 0\n\t"
               "s_cbranch_scc0 2f\n\ts_sleep 1\n\ts_branch 1b\n2:" :: "s"(__builtin_amdgcn_readfirstlane(t)) : "vcc", "scc", "memory");
  SB();
}
__device__ __forceinline__ void pace(unsigned t, int& lprev, unsigned slip_addr) {
  unsigned vt, slip, tgt;
  int lp = __builtin_amdgcn_readfirstlane(lprev);
  SB();
  asm volatile(
      "ds_read_b32 %0, %5\n\ts_memrealtime vcc\n\ts_waitcnt lgkmcnt(0)\n\t"
      "v_readfirstlane_b32 %1, %0\n\t"
      "s_add_u32 %2, %1, %4\n\t"
      "s_sub_u32 vcc_lo, vcc_lo, %2\n\t"
      "s_lshl_b32 %3, %3, 4\n\ts_sub_i32 vcc_hi, vcc_lo, %3\n\t"
      "s_cmp_gt_i32 vcc_hi, %7\n\t"
      "s_cbranch_scc0 3f\n\t"
      "s_add_u32 %1, %1, %6\n\ts_add_u32 %2, %2, %6\n\ts_sub_u32 vcc_lo, vcc_lo, %6\n\t"
      "v_mov_b32 %0, %1\n\tds_max_u32 %5, %0\n"
      "3:\n\ts_max_i32 %3, vcc_lo, 0\n"
      "1:\n\ts_cmp_lt_i32 vcc_lo, 0\n\ts_cbranch_scc0 2f\n\ts_sleep 1\n\t"
      "s_memrealtime vcc\n\ts_waitcnt lgkmcnt(0)\n\ts_sub_u32 vcc_lo, vcc_lo, %2\n\ts_branch 1b\n"
      "2:\n\ts_waitcnt lgkmcnt(0)"
      : "=&v"(vt), "=&s"(slip), "=&s"(tgt), "+s"(lp)
      : "s"(__builtin_amdgcn_readfirstlane(t)), "v"(slip_addr), "i"((SLIP_N * 4 * PACE_PER + 128) >> 8), "i"(SLIP_THR)
      : "vcc", "scc", "memory");
  lprev = __builtin_amdgcn_readfirstlane(lp);
  SB();
}

__global__ void __launch_bounds__(256, 1) decode_fused(Params args) {
  __shared__ __attribute__((aligned(16))) unsigned char smem[L_BYTES];
  LAS unsigned char* L = (LAS unsigned char*)smem;
  v8p arg_a;
  v4p arg_b;
  unsigned long long clk0;
  asm volatile("s_load_dwordx16 %0, %3, 0x0\n\ts_load_dwordx8 %1, %3, 0x40\n\ts_memrealtime %2"
               : "=&s"(arg_a), "=&s"(arg_b), "=&s"(clk0) : "s"(__builtin_amdgcn_kernarg_segment_ptr()));
  SB();
  const int bid = blockIdx.x, kvh = bid >> 5, b = (bid >> 3) & 3, sp = bid & 7;
  const int br = bid >> 7, j = (bid >> 3) & 15, kr = (sp + 4 * br) & 7, cb = j & 3, rb = j >> 2;
  const int tid = threadIdx.x, lane = tid & 63, w = __builtin_amdgcn_readfirstlane(tid >> 6);
  const int voff = lane * 16, koff = tid * 16;
  const bool b3 = lane & 8, b2 = lane & 4, b1 = lane & 2, b0 = lane & 1;
  LAS float* const sc = (LAS float*)(L + L_SC + tid * 4);

  unsigned cpf;
  {
    unsigned long long pc;
    unsigned len;
    asm volatile("s_getpc_b64 %0\n.Ldec_code_begin:\n\ts_mov_b32 %1, .Ldec_code_end-.Ldec_code_begin" : "=s"(pc), "=s"(len));
    cpf = __builtin_amdgcn_raw_buffer_load_b32(__builtin_amdgcn_make_buffer_rsrc((void*)pc, 0, len, RSRC_W3), tid < 8 ? tid * 128 : OOB, (bid >> 3) * 1024, 0);
  }
  const int row0 = 128 * w + 8 * (lane >> 2);
  int xoff = row0 < DM ? row0 * 2 : OOB, woff = row0 * (NQ * 2) + (lane & 3) * 16, wso = (kr * 512 + j * 32) * 2;
  int poffv = w == 3 && lane < 56 ? (lane >> 1) * (16 * NQ * 2) + (lane & 1) * 64 + kr * 1024 + j * (NQ * 2) : OOB;
  int eso = w == 0 ? WS_EPOCH : OOB;
  const int f2 = wso + 447 * (NQ * 2), f3 = WS_FREE + (bid >> 2) * 384, f01 = (j & 1) ? wso : OOB, f23 = (j & 1) ? f3 : f2;
  int fso = (j & 2) ? f23 : f01;
  asm volatile("" : "+v"(xoff), "+v"(woff), "+v"(poffv));
  SB();
  asm volatile("s_waitcnt lgkmcnt(0)" : "+s"(arg_a), "+s"(arg_b), "+s"(clk0), "+s"(wso), "+s"(eso), "+s"(fso) :: "memory");
  SB();
  Params p;
  p.x0 = (void*)arg_a[0]; p.k0 = (void*)arg_a[1]; p.v0 = (void*)arg_a[2]; p.wq0 = (void*)arg_a[3]; p.wo0 = (void*)arg_a[4];
  p.x1 = (void*)arg_a[5]; p.k1 = (void*)arg_a[6]; p.v1 = (void*)arg_a[7]; p.wq1 = (void*)arg_b[0]; p.wo1 = (void*)arg_b[1];
  p.out = (void*)arg_b[2]; p.ws = (unsigned char*)arg_b[3];
  unsigned t0 = (unsigned)clk0;
  const auto rws = __builtin_amdgcn_make_buffer_rsrc(p.ws, 0, WS_END, RSRC_W3);

  v4u X[4], W[8], X0[16], X1[16], R[16];
  unsigned pfw, ft;
  unsigned ep;
  {
    const auto rx = __builtin_amdgcn_make_buffer_rsrc((void*)(br ? p.x1 : p.x0), 0, 4 * DM * 2, RSRC_W3);
    const auto rwq = __builtin_amdgcn_make_buffer_rsrc((void*)(br ? p.wq1 : p.wq0), 0, DM * NQ * 2, RSRC_W3);
    {
      const size_t wqp = (size_t)(br ? p.wq1 : p.wq0), a = (j & 3) == 3 ? (size_t)p.ws : wqp;
      ft = __builtin_amdgcn_raw_buffer_load_b32(__builtin_amdgcn_make_buffer_rsrc((void*)a, 0, DM * NQ * 2, RSRC_W3), 0, fso, AUX_NT);
      SB();
    }
#pragma unroll
    for (int i = 0; i < 4; ++i) X[i] = __builtin_amdgcn_raw_buffer_load_b128(rx, xoff + i * DM * 2, 0, 0);
    pfw = __builtin_amdgcn_raw_buffer_load_b32(rwq, poffv, (384u - (unsigned)(size_t)(br ? p.wq1 : p.wq0)) & 896u, AUX_NT);
    SB();
#pragma unroll
    for (int n = 0; n < 8; ++n) W[n] = __builtin_amdgcn_raw_buffer_load_b128(rwq, woff, wso + n * (NQ * 2), AUX_NT);
    SB();
    ep = __builtin_amdgcn_raw_buffer_load_b32(rws, 0, eso, AUX_SC1);
    SB();
  }
  asm volatile("" : "+s"(p.wo0), "+s"(p.wo1), "+s"(p.v1));
  const size_t reg_off = (size_t)(b * 8 + kvh) * REGION_BYTES + sp * (REGION_BYTES / 8);
  const char* const wop = (const char*)(br ? p.wo1 : p.wo0) + (kr * 512 + rb * 128) * (DM * 2) + cb * 256;
#define LDU(rs, i) __builtin_amdgcn_raw_buffer_load_b128(rs, koff, (i) * 4096, AUX_NT)
  const int poff = PACE_OFF * 256 + (((b * 8 + kvh) * 19) & 31) * (4 * PACE_PER / 32);
  const auto rk0 = __builtin_amdgcn_make_buffer_rsrc((char*)p.k0 + reg_off, 0, 48 * 4096, RSRC_W3);
#define ISSUE(lo, hi)                                                                     \
  _Pragma("unroll") for (int i_ = (lo); i_ < (hi) && i_ < 48; ++i_) {                     \
    if (i_ >= H_A + 4 && (i_ & 3) == 0) wait_until(t0 + ((i_ * PACE_PER + poff) >> 8));   \
    if (i_ < 16) X0[i_ & 15] = LDU(rk0, i_);                                              \
    else if (i_ < 32) X1[i_ & 15] = LDU(rk0, i_);                                         \
    else R[i_ & 15] = LDU(rk0, i_);                                                       \
    SB();                                                                                 \
  }
  ISSUE(0, H_A)

  {
    float a[4][8];
#pragma unroll
    for (int bb = 0; bb < 4; ++bb)
#pragma unroll
      for (int c = 0; c < 8; ++c) a[bb][c] = 0.f;
#pragma unroll
    for (int t = 0; t < 4; ++t) {
#pragma unroll
      for (int k = 0; k < 4; ++k) {
        const unsigned lo = pk_lo(W[2 * t][k], W[2 * t + 1][k]), hi = pk_hi(W[2 * t][k], W[2 * t + 1][k]);
#pragma unroll
        for (int bb = 0; bb < 4; ++bb) {
          a[bb][2 * k] = dot2(lo, X[bb][t], a[bb][2 * k]);
          a[bb][2 * k + 1] = dot2(hi, X[bb][t], a[bb][2 * k + 1]);
        }
      }
      SB();
      ISSUE(H_A + t, H_A + t + 1)
    }
    float y[8];
#pragma unroll
    for (int c = 0; c < 8; ++c) {
      y[c] = sum16(sum32(a[0][c], a[2][c]), sum32(a[1][c], a[3][c]));
      y[c] += dppf<ROR8>(y[c]);
      y[c] += dppf<ROR4>(y[c]);
    }
    LAS f32x4* qp = (LAS f32x4*)(L + L_QP + w * 512 + (lane >> 4) * 128 + (lane & 3) * 32);
    qp[0] = f32x4{y[0], y[1], y[2], y[3]};
    qp[1] = f32x4{y[4], y[5], y[6], y[7]};
  }
  *(LAS unsigned*)(L + (w == 0 ? L_ML : L_O)) = ep;
  LDS_BARRIER();
  const unsigned tag = __builtin_amdgcn_readfirstlane(*(volatile LAS unsigned*)(L + L_ML)) + 1u;
  if (w == 0) {
    const unsigned tt = ((NOW() >> 2) & 0xffu) | tag << 8;
    const LAS f32x4* qp = (const LAS f32x4*)(L + L_QP + (lane & 31) * 16);
    const f32x4 q = (qp[0] + qp[32]) + (qp[64] + qp[96]);
    const unsigned lo = cvtpk(q.x, q.y), hi = cvtpk(q.z, q.w);
    __builtin_amdgcn_raw_buffer_store_b128(v4u{pk_lo(lo, tt), __builtin_amdgcn_alignbit(tt + (TAG_INC << 8), lo, 16), pk_lo(hi, tt + (2 * TAG_INC << 8)),
                                               __builtin_amdgcn_alignbit(tt + (3 * TAG_INC << 8), hi, 16)},
                                           rws, lane < 32 ? voff : OOB, WS_QG + (kr * 32 + br * 16 + j) * 512, AUX_SC1);
  }
  SB();
  ISSUE(H_A + 4, H_A + 8)
  unsigned tch;
  {
    const size_t a12 = w == 1 ? (size_t)p.v0 : (size_t)p.k1, a03 = w == 3 ? (size_t)p.v1 : (size_t)wop;
    const size_t a = (w == 1 || w == 2 ? a12 : a03) + (w ? reg_off : 0);
    tch = __builtin_amdgcn_raw_buffer_load_b32(__builtin_amdgcn_make_buffer_rsrc((void*)a, 0, 65536, RSRC_W3), lane == 0 ? 0 : OOB,
                                               ((bid * 3 + w) & 63) * 896, AUX_NT);
    SB();
  }
  ISSUE(H_A + 8, H_P)
  int budget;
  {
    const int qg_off = WS_QG + kvh * 32 * 512 + b * 128, qg_v = (tid >> 3) * 512 + (tid & 7) * 16;
    const int ck_v = w == 0 ? (lane & 15) * (16 * 512) + 4 * 512 : OOB;
    const unsigned ck_mask = w == 0 ? ~0u : 0u;
    v4u qg = __builtin_amdgcn_raw_buffer_load_b128(rws, qg_v, qg_off, AUX_SC1);
    v4u ck = __builtin_amdgcn_raw_buffer_load_b128(rws, ck_v, WS_QG, AUX_SC1);
    SB();
    ISSUE(H_P, H_C)
    KEEP(cpf);
    KEEP(ft);
    KEEP(pfw);
    if (__any((diff4(qg, tag) | (diff4(ck, tag) & ck_mask)) >> 24)) {
#pragma unroll 1
      for (unsigned spins = 0; spins < SPIN_CAP; ++spins) {
        MEMFENCE();
        qg = __builtin_amdgcn_raw_buffer_load_b128(rws, qg_v, qg_off, AUX_SC1);
        ck = __builtin_amdgcn_raw_buffer_load_b128(rws, ck_v, WS_QG, AUX_SC1);
        if (!__any((diff4(qg, tag) | (diff4(ck, tag) & ck_mask)) >> 24)) break;
        __builtin_amdgcn_s_sleep(2);
      }
    }
    *(LAS v2u*)(L + L_Q + tid * 8) = v2u{pk_lo(qg.x, qg.y), pk_lo(qg.z, qg.w)};
    const unsigned ref = (t0 + PUB_NOM) & ~3u;
    const float d = (float)(int)(signed char)((ck.x >> 16) - (ref >> 2));
    const int mean = (int)row_sum(d) >> 2, skew = (int)row_max(d) * 4 - mean;
    *(LAS v4u*)(L + (w == 0 ? L_SLIP : L_O)) = v4u{0u, ref + (unsigned)mean - PUB_NOM, (unsigned)skew, 0u};
  }
  LDS_BARRIER();
  {
    const v4u an = *(const LAS v4u*)(L + L_SLIP);
    t0 = __builtin_amdgcn_readfirstlane(t0 + min(max((int)(an.y - t0), -150), 400));
    budget = __builtin_amdgcn_readfirstlane(EARLY + ((min(max((int)an.z - SK_0, 0), 200) * SK_K) >> 2));
  }
  v4u q0 = *(const LAS v4u*)(L + L_Q + (lane & 31) * 16), q1 = *(const LAS v4u*)(L + L_Q + 512 + (lane & 31) * 16);

  float acc[2][8], m = -INFINITY, l = 0.f;
#pragma unroll
  for (int k = 0; k < 8; ++k) acc[0][k] = acc[1][k] = 0.f;
#define QK_STEP(KR, i)                                                 \
  {                                                                    \
    float s0 = dot2(KR[i][0], q0[0], 0.f), s1 = dot2(KR[i][0], q1[0], 0.f); \
    s0 = dot2(KR[i][1], q0[1], s0); s1 = dot2(KR[i][1], q1[1], s1);    \
    s0 = dot2(KR[i][2], q0[2], s0); s1 = dot2(KR[i][2], q1[2], s1);    \
    s0 = dot2(KR[i][3], q0[3], s0); s1 = dot2(KR[i][3], q1[3], s1);    \
    y[i] = sum16(s0, s1);                                              \
  }
#define TR_STEP(H, CTRL, BIT)                                          \
  _Pragma("unroll") for (int i = 0; i < (H); ++i) {                    \
    const float keep = (BIT) ? y[i + (H)] : y[i], send = (BIT) ? y[i] : y[i + (H)]; \
    y[i] = keep + dppf<CTRL>(send);                                    \
  }
#define SCORES(slot)                                                   \
  TR_STEP(8, ROR8, b3) TR_STEP(4, XOR7, b2) TR_STEP(2, XOR2, b1) TR_STEP(1, XOR1, b0) \
  m = fmaxf(m, y[0]);                                                  \
  sc[(slot) * 256] = y[0];
#define PROBS(slot)                                                                     \
  const float pp = __builtin_amdgcn_exp2f((sc[(slot) * 256] - m) * SCL);                \
  l += pp;                                                                              \
  const unsigned ppk = cvtpk(pp, dppf<XOR1>(pp));        \
  const auto ph = __builtin_amdgcn_permlane16_swap(ppk, ppk, false, false);
#define PV_STEP(t)                                                     \
  {                                                                    \
    const unsigned p0 = dppu<BCAST + 2 * (t)>(ph[0]), p1 = dppu<BCAST + 2 * (t)>(ph[1]); \
    _Pragma("unroll") for (int k = 0; k < 4; ++k) {                    \
      const unsigned lo = pk_lo(R[2 * (t)][k], R[2 * (t) + 1][k]), hi = pk_hi(R[2 * (t)][k], R[2 * (t) + 1][k]); \
      acc[0][2 * k] = dot2(lo, p0, acc[0][2 * k]); acc[0][2 * k + 1] = dot2(hi, p0, acc[0][2 * k + 1]); \
      acc[1][2 * k] = dot2(lo, p1, acc[1][2 * k]); acc[1][2 * k + 1] = dot2(hi, p1, acc[1][2 * k + 1]); \
    }                                                                  \
  }
#define DUMP(brn)                                                      \
  {                                                                    \
    float o[8];                                                        \
    _Pragma("unroll") for (int k = 0; k < 8; ++k) o[k] = sum32(acc[0][k], acc[1][k]);     \
    LAS f32x4* mp = (LAS f32x4*)(L + L_MRG + (brn) * 8192 + tid * 32); \
    mp[0] = f32x4{o[0], o[1], o[2], o[3]};                             \
    mp[1] = f32x4{o[4], o[5], o[6], o[7]};                             \
  }
#define DUMP_ML(brn)                                                   \
  l = row_sum(l);                                                      \
  l = sum32(l, l);                                                     \
  *(LAS f32x2*)(L + L_ML + (brn) * 64 + w * 16 + ((lane >> 4) & 1) * 8) = f32x2{m, l};
  {
    float y[16];
#pragma unroll
    for (int i = 0; i < 16; ++i) {
      if (i % H_S == 0) { SB(); ISSUE(H_C + (i / H_S) * 4, H_C + (i / H_S) * 4 + 4) }
      QK_STEP(X0, i)
    }
    SCORES(0)
#pragma unroll
    for (int i = 0; i < 16; ++i) {
      if ((i + 16) % H_S == 0) { SB(); ISSUE(H_C + ((i + 16) / H_S) * 4, H_C + ((i + 16) / H_S) * 4 + 4) }
      QK_STEP(X1, i)
    }
    SCORES(1)
    SB();
  }
  ISSUE(H_C + (31 / H_S + 1) * 4, 48)
  KEEP(tch);
  {
    const unsigned slip_addr = (unsigned)(unsigned long)(L + L_SLIP);
    int lprev = 1 << 20;
    int xt = 48 * PACE_PER + poff;
#define BURST(g)                                                       \
  { const int inc_ = min(budget, EARLY_STEP); t0 += inc_; budget -= inc_; } \
  pace(t0 + (xt >> 8), lprev, slip_addr);                              \
  xt += 4 * PACE_PER;                                                  \
  _Pragma("unroll") for (int u_ = 4 * (g); u_ < 4 * (g) + 4; ++u_) { R[u_] = LDU(rn, u_); SB(); }
    const char *kp = (const char*)p.k0 + reg_off, *vp = (const char*)p.v0 + reg_off;
    int g = 2;
#pragma unroll 1
    for (int grp = 0;; ++grp) {
#pragma unroll 1
      do {
        const auto rn = __builtin_amdgcn_make_buffer_rsrc((void*)(g < 7 ? kp + (g + 1) * 65536 : vp), 0, 65536, RSRC_W3);
        float y[16];
#pragma unroll
        for (int i = 0; i < 16; ++i) {
          QK_STEP(R, i)
          if ((i & 3) == 3) { BURST(i >> 2) }
        }
        SCORES(g)
        SB();
      } while (++g < 8);
      m = max32(row_max(m));
      kp = (const char*)p.k1 + reg_off;
      g = 0;
#pragma unroll 1
      do {
        const auto rn = __builtin_amdgcn_make_buffer_rsrc((void*)(g < 7 ? vp + (g + 1) * 65536 : kp), 0, 65536, RSRC_W3);
        PROBS(g)
        PV_STEP(0) PV_STEP(1) BURST(0) PV_STEP(2) PV_STEP(3) BURST(1) PV_STEP(4) PV_STEP(5) BURST(2) PV_STEP(6) PV_STEP(7) BURST(3)
      } while (++g < 8 - grp);
      if (grp) break;
      DUMP(0)
      DUMP_ML(0)
#pragma unroll
      for (int k = 0; k < 8; ++k) acc[0][k] = acc[1][k] = 0.f;
      m = -INFINITY;
      l = 0.f;
      q0 = *(const LAS v4u*)(L + L_Q + 1024 + (lane & 31) * 16);
      q1 = *(const LAS v4u*)(L + L_Q + 1536 + (lane & 31) * 16);
      vp = (const char*)p.v1 + reg_off;
      g = 0;
    }
  }
  const unsigned t_end = t0 + __builtin_amdgcn_readfirstlane(*(volatile LAS unsigned*)(L + L_SLIP)) + ((512 * PACE_PER + PACE_OFF * 256) >> 8);
  v4u WO[8];
  const int ooff = cb < 3 || !b3 ? (lane >> 4) * (DM * 2) + (lane & 15) * 16 : OOB;
#define WO_LOADS(lo, hi)                                                                  \
  SB();                                                                                   \
  _Pragma("unroll") for (int n = (lo); n < (hi); ++n)                                     \
    WO[n] = __builtin_amdgcn_raw_buffer_load_b128(__builtin_amdgcn_make_buffer_rsrc((void*)wop, 0, 128 * DM * 2 - cb * 256, RSRC_W3), ooff, \
                                                  (32 * w + 4 * n) * (DM * 2), AUX_NT);   \
  SB();
  int a_ml = L_ML + (w >> 1) * 64 + (w & 1) * 8, a_mrg = L_MRG + (w >> 1) * 8192 + (w & 1) * 1024 + voff;
  int og = WS_OG + ((w >> 1) * 256 + bid) * OG_STR + (w & 1) * 1024, ogm = WS_OG + ((w >> 1) * 256 + bid) * OG_STR + 2048 + (w & 1) * 16;
  unsigned tg1 = tag + TAG_INC, tg2 = tag + 2 * TAG_INC, tg3 = tag + 3 * TAG_INC;
  asm volatile("" : "+v"(a_ml), "+v"(a_mrg));
  asm volatile("" : "+s"(og), "+s"(ogm), "+s"(tg1), "+s"(tg2), "+s"(tg3));
#define ENC4(f) v4u{enc_f(f.x, tag), enc_f(f.y, tg1), enc_f(f.z, tg2), enc_f(f.w, tg3)}
  {
    PROBS(7)
    DUMP_ML(1)
    SB();
    PV_STEP(0) PV_STEP(1) PV_STEP(2) PV_STEP(3) PV_STEP(4) PV_STEP(5) PV_STEP(6) PV_STEP(7)
    WO_LOADS(0, WO_S)
  }

  DUMP(1)
  LDS_BARRIER();
  {
    f32x2 ml[4];
    f32x4 ov[4];
#pragma unroll
    for (int i = 0; i < 4; ++i) {
      ml[i] = *(const LAS f32x2*)(L + a_ml + i * 16);
      ov[i] = *(const LAS f32x4*)(L + a_mrg + i * 2048);
    }
    const float M = fmaxf(fmaxf(ml[0].x, ml[1].x), fmaxf(ml[2].x, ml[3].x));
    f32x4 o = {0.f, 0.f, 0.f, 0.f};
    float ls = 0.f;
#pragma unroll
    for (int i = 0; i < 4; ++i) {
      const float e = __builtin_amdgcn_exp2f((ml[i].x - M) * SCL);
      o += e * ov[i];
      ls = fmaf(e, ml[i].y, ls);
    }
    __builtin_amdgcn_raw_buffer_store_b128(ENC4(o), rws, voff, og, AUX_SC1);
    __builtin_amdgcn_raw_buffer_store_b128(v4u{enc_f(M, tag), (__float_as_uint(M) & 0xffu) | tag << 8, enc_f(ls, tag), tag}, rws, lane == 0 ? 0 : OOB, ogm, AUX_SC1);
  }
  WO_LOADS(WO_S, 8)
  int og_v = (lane >> 5) * OG_STR + (lane & 31) * 16;
  int ml_v = (((lane & 3) * 2 + (lane >> 5)) ^ ((lane >> 2) & 1)) * OG_STR;
  int a_ow = L_O + w * 256 + (lane & 31) * 8 + (lane >> 5) * 4, a_or = L_O + (lane & 3) * 256 + (lane >> 4) * 16;
  int pg_v = b3 || (cb == 3 && w >= 2) ? OOB : (lane & 3) * 4096 + (lane & 4) * 16 + (lane >> 4) * 16;
  int go[5], pg_s = WS_PG + ((br * 4 + cb) * 4 + w) * 16384 + (kr * 4 + rb) * 128;
#pragma unroll
  for (int s = 0; s < 5; ++s) go[s] = WS_OG + (br * 256 + kr * 32 + w * 8) * OG_STR + (s < 4 ? rb * 512 + s * 2 * OG_STR : 2048 + (rb >> 1) * 16);
  asm volatile("" : "+v"(og_v), "+v"(ml_v), "+v"(a_ow), "+v"(a_or), "+v"(pg_v));
  asm volatile("" : "+s"(go[0]), "+s"(go[1]), "+s"(go[2]), "+s"(go[3]), "+s"(go[4]), "+s"(pg_s));
  wait_until(t_end + T_GATHER - (((31 - (((w * 8 + kr) * 19) & 31)) * G_SPREAD) >> 5));

  v4u BW[2][4];
#define BW_READS()                                                                        \
  _Pragma("unroll") for (int ks = 0; ks < 4; ++ks) _Pragma("unroll") for (int c = 0; c < 2; ++c) { \
    LAS unsigned char* a = L + L_WO + (32 * ks + 8 * (lane >> 4) + ((lane >> 2) & 3)) * WO_STR + (2 * w + c) * 32 + (lane & 3) * 8; \
    const v2u r0 = __builtin_bit_cast(v2u, __builtin_amdgcn_ds_read_tr16_b64_v4i16((LAS v4s*)a));  \
    const v2u r1 = __builtin_bit_cast(v2u, __builtin_amdgcn_ds_read_tr16_b64_v4i16((LAS v4s*)(a + 4 * WO_STR))); \
    BW[c][ks] = v4u{r0.x, r0.y, r1.x, r1.y};                                              \
  }
  {
    v4u g[4], gm;
#define G_BAD() (((diff2(gm.x, gm.z, tag << 24) | diff16(g, tag)) >> 24) | \
                 ((gm.y ^ (tag << 8)) >> 8) | (gm.w ^ tag))
#define GATHER()                                                                                          \
  gm = __builtin_amdgcn_raw_buffer_load_b128(rws, ml_v, go[4], AUX_SC1);                                  \
  _Pragma("unroll") for (int s = 0; s < 4; ++s) g[s] = __builtin_amdgcn_raw_buffer_load_b128(rws, og_v, go[s], AUX_SC1);
    GATHER()
    SB();
#pragma unroll
    for (int n = 0; n < 8; ++n) *(LAS v4u*)(L + L_WO + (32 * w + 4 * n + (lane >> 4)) * WO_STR + (lane & 15) * 16) = WO[n];
    LDS_BARRIER();
    BW_READS()
    SB();
    if (__any(G_BAD())) {
#pragma unroll 1
      for (unsigned spins = 0; spins < SPIN_CAP; ++spins) {
        MEMFENCE();
        GATHER()
        if (!__any(G_BAD())) break;
        __builtin_amdgcn_s_sleep(1);
      }
    }
    const float Ms = __uint_as_float(gm.x << 8 | (gm.y & 0xffu)), Ls = dec_f(gm.z);
    float Mx = fmaxf(Ms, dppf<XOR1>(Ms));
    Mx = fmaxf(Mx, dppf<XOR2>(Mx));
    Mx = fmaxf(Mx, dppf<XOR7>(Mx));
    const float e = __builtin_amdgcn_exp2f((Ms - Mx) * SCL);
    float Lt = e * Ls;
    Lt += dppf<XOR1>(Lt);
    Lt += dppf<XOR2>(Lt);
    Lt += dppf<XOR7>(Lt);
    const float c = e * __builtin_amdgcn_rcpf(Lt);
    float o0 = 0.f, o1 = 0.f, o2 = 0.f, o3 = 0.f;
#define MRG(s) { const float cs = dppf<BCAST + s>(c); o0 = fmaf(cs, dec_f(g[s].x), o0); o1 = fmaf(cs, dec_f(g[s].y), o1); o2 = fmaf(cs, dec_f(g[s].z), o2); o3 = fmaf(cs, dec_f(g[s].w), o3); }
    MRG(0) MRG(1) MRG(2) MRG(3)
    *(LAS unsigned*)(L + a_ow) = cvtpk(sum32(o0, o2), sum32(o1, o3));
  }
  LDS_BARRIER();
  {
    v4u o[4];
#pragma unroll
    for (int ks = 0; ks < 4; ++ks) o[ks] = *(const LAS v4u*)(L + a_or + ks * 64);
    SB();
    f32x4 d0 = {0.f, 0.f, 0.f, 0.f}, d1 = {0.f, 0.f, 0.f, 0.f};
#pragma unroll
    for (int ks = 0; ks < 4; ++ks) {
      d0 = __builtin_amdgcn_mfma_f32_16x16x32_bf16(__builtin_bit_cast(bf16x8, BW[0][ks]), __builtin_bit_cast(bf16x8, o[ks]), d0, 0, 0, 0);
      d1 = __builtin_amdgcn_mfma_f32_16x16x32_bf16(__builtin_bit_cast(bf16x8, BW[1][ks]), __builtin_bit_cast(bf16x8, o[ks]), d1, 0, 0, 0);
    }
    const f32x4 d = b2 ? d1 : d0;
    __builtin_amdgcn_raw_buffer_store_b128(ENC4(d), rws, pg_v, pg_s, AUX_SC1);
  }

  if (w == 3 && (bid & 1) == 0 && (bid & 0x70) < 0x70) {
    const int ri = bid >> 1;
    v4u g[4];
    int o_v = (lane & 24) == 0 ? (lane & 7) * 8 + (lane >> 5) * 4 : OOB;
    int ps = WS_PG + ri * 4096, o_s = ((ri & 3) * 2 * DM + (ri >> 6) * DM + ((ri >> 2) & 15) * 32) * 2;
    asm volatile("" : "+v"(o_v));
    asm volatile("" : "+s"(ps), "+s"(o_s));
    DRAIN();
    wait_until(t_end + T_REDUCE);
#pragma unroll 1
    for (unsigned spins = 0; spins < SPIN_CAP; ++spins) {
      MEMFENCE();
#pragma unroll
      for (int s = 0; s < 4; ++s) g[s] = __builtin_amdgcn_raw_buffer_load_b128(rws, voff + s * 1024, ps, AUX_SC1);
      if (!__any(diff16(g, tag) >> 24)) break;
      __builtin_amdgcn_s_sleep(1);
    }
    float sum[4];
#pragma unroll
    for (int k = 0; k < 4; ++k) sum[k] = (dec_f(g[0][k]) + dec_f(g[1][k])) + (dec_f(g[2][k]) + dec_f(g[3][k]));
    const auto ro = __builtin_amdgcn_make_buffer_rsrc(p.out, 0, 4 * 2 * DM * 2, RSRC_W3);
    float s01 = sum16(sum32(sum[0], sum[2]), sum32(sum[1], sum[3]));
    s01 += dppf<ROR8>(s01);
    const unsigned pk = cvtpk(s01, s01);
    const auto ev = __builtin_amdgcn_permlane16_swap(pk, pk, false, false);
    __builtin_amdgcn_raw_buffer_store_b32(pk_lo(ev[0], ev[1]), ro, o_v, o_s, AUX_SC1);
    __builtin_amdgcn_raw_buffer_store_b32(tag, rws, (bid == 0 && lane == 0) ? 0 : OOB, WS_EPOCH, AUX_SC1);
  }
  asm volatile(".Ldec_code_end:");
}

}

extern "C" void kernel_launch(void* const* d_in, const int* in_sizes, int n_in, void* d_out, int out_size, void* d_ws, size_t ws_size,
                              hipStream_t stream) {
  Params p;
  p.x0 = d_in[0]; p.k0 = d_in[1]; p.v0 = d_in[2]; p.wq0 = d_in[3]; p.wo0 = d_in[4];
  p.x1 = d_in[5]; p.k1 = d_in[6]; p.v1 = d_in[7]; p.wq1 = d_in[8]; p.wo1 = d_in[9];
  p.out = d_out;
  p.ws = (unsigned char*)d_ws;
  decode_fused<<<256, 256, 0, stream>>>(p);
}
```
